```python
import torch

def _softmax(x, axis=-1):
    return torch.softmax(x, dim=axis)
def _gelu(x):
    return torch.nn.functional.gelu(x, approximate="tanh")
def _silu(x):
    return torch.nn.functional.silu(x)
def _sigmoid(x):
    return torch.sigmoid(x)
def _layernorm(x, axis=-1, eps=1e-5):
    m = x.mean(dim=axis, keepdim=True)
    v = x.var(dim=axis, keepdim=True, unbiased=False)
    return (x - m) / torch.sqrt(v + eps)
def _scatter_add(tgt, idx, src, axis=0):
    return tgt.clone().index_add_(axis, idx, src)
def _segment_sum(src, seg, n):
    out = torch.zeros((n,) + src.shape[1:], dtype=src.dtype)
    return out.index_add_(0, seg, src)
def _pad(x, pw):
    flat = tuple(v for pair in reversed(pw) for v in pair)
    return torch.nn.functional.pad(x, flat)

kernel_name = 'parallel_sum__spectral_gfnet__mixer_token_mlp__prompt'

def setup_inputs():

    _w = (lambda a: (lambda t: t.to(torch.bfloat16) if t.is_floating_point() else t)(torch.as_tensor(a)))
    def _A_setup():
        B, S, D = 1, 2048, 1024
        Df = D // 2 + 1
        return {
            "x": _w((lambda *s: torch.randn(*s, dtype=torch.bfloat16) * 0.05)(B, S, D)),
            "w_r": _w((lambda *s: torch.randn(*s, dtype=torch.bfloat16) * 1.6)(S, Df) * 0.02),
            "w_i": _w((lambda *s: torch.randn(*s, dtype=torch.bfloat16) * 1.6)(S, Df) * 0.02),
        }
    _A_in = _A_setup()

    def _B_setup():
        B, S, D, F = 1, 2048, 1024, 3584
        return {
            "x": _w((lambda *s: torch.randn(*s, dtype=torch.bfloat16) * 0.05)(B, S, D)),
            "w1": _w((lambda *s: torch.randn(*s, dtype=torch.bfloat16) * 0.4)(S, F) * (S ** -0.5)),
            "b1": _w((lambda *s: torch.randn(*s, dtype=torch.bfloat16) * 3.2)(F) * 0.02),
            "w2": _w((lambda *s: torch.randn(*s, dtype=torch.bfloat16) * 0.8)(F, S) * (F ** -0.5)),
            "b2": _w((lambda *s: torch.randn(*s, dtype=torch.bfloat16) * 3.2)(S) * 0.02),
        }
    _B_in = _B_setup()

    return {
        "A_x": _A_in["x"],
        "A_w_r": _A_in["w_r"],
        "A_w_i": _A_in["w_i"],
        "B_x": _B_in["x"],
        "B_w1": _B_in["w1"],
        "B_b1": _B_in["b1"],
        "B_w2": _B_in["w2"],
        "B_b2": _B_in["b2"],
    }

def reference(A_x, A_w_r, A_w_i, B_x, B_w1, B_b1, B_w2, B_b2):
    def _A_ref(x, w_r, w_i):
        B, S, D = x.shape
        X = (lambda x, axes=(-2,-1): torch.fft.rfft2(x.float(), dim=axes))(x, axes=(-2, -1))
        W = w_r + 1j * w_i
        X = X * W
        y = (lambda x, s, axes=(-2,-1): torch.fft.irfft2(x, s=s, dim=axes).to(torch.bfloat16))(X, (S, D), axes=(-2, -1))
        return y

    def _B_ref(x, w1, b1, w2, b2):
        B, S, D = x.shape
        h = _layernorm(x, axis=-1)
        u = torch.einsum("bsd,sf->bdf", h, w1) + b1
        u = _gelu(u)
        y = torch.einsum("bdf,fs->bds", u, w2) + b2
        return (lambda x, *d: x.permute(*d))(y, 0, 2, 1)

    x = A_x
    xn = _layernorm(x, axis=-1)
    return x + _A_ref(xn, A_w_r, A_w_i) + _B_ref(xn, B_w1, B_b1, B_w2, B_b2)
```

```cpp
#include <hip/hip_runtime.h>
#include <hip/hip_bf16.h>

#define KLOOP_TA \
  "ds_read_b64_tr_b16 v[144:145], %[ta0] offset:0\n\t" \
  "ds_read_b64_tr_b16 v[146:147], %[ta0] offset:4096\n\t" \
  "ds_read_b128 v[128:131], %[pa] offset:0\n\t" \
  "ds_read_b128 v[132:135], %[pa] offset:256\n\t" \
  "ds_read_b128 v[136:139], %[pa] offset:512\n\t" \
  "ds_read_b128 v[140:143], %[pa] offset:768\n\t" \
  "ds_read_b64_tr_b16 v[148:149], %[ta1] offset:0\n\t" \
  "ds_read_b64_tr_b16 v[150:151], %[ta1] offset:4096\n\t" \
  "ds_read_b64_tr_b16 v[152:153], %[ta2] offset:0\n\t" \
  "ds_read_b64_tr_b16 v[154:155], %[ta2] offset:4096\n\t" \
  "ds_read_b64_tr_b16 v[156:157], %[ta3] offset:0\n\t" \
  "ds_read_b64_tr_b16 v[158:159], %[ta3] offset:4096\n\t" \
  "s_mov_b32 %[cnt], %[nq]\n\t" \
  ".p2align 6\n\t" \
  "1:\n\t" \
  "s_waitcnt lgkmcnt(0)\n\t" \
  "v_mfma_f32_16x16x32_bf16 v[64:67], v[144:147], v[128:131], v[64:67]\n\t" \
  "ds_read_b64_tr_b16 v[176:177], %[ta0] offset:8192\n\t" \
  "v_mfma_f32_16x16x32_bf16 v[68:71], v[144:147], v[132:135], v[68:71]\n\t" \
  "ds_read_b64_tr_b16 v[178:179], %[ta0] offset:12288\n\t" \
  "v_mfma_f32_16x16x32_bf16 v[72:75], v[144:147], v[136:139], v[72:75]\n\t" \
  "ds_read_b128 v[160:163], %[pa] offset:8192\n\t" \
  "v_mfma_f32_16x16x32_bf16 v[76:79], v[144:147], v[140:143], v[76:79]\n\t" \
  "ds_read_b128 v[164:167], %[pa] offset:8448\n\t" \
  "v_mfma_f32_16x16x32_bf16 v[80:83], v[148:151], v[128:131], v[80:83]\n\t" \
  "ds_read_b128 v[168:171], %[pa] offset:8704\n\t" \
  "v_mfma_f32_16x16x32_bf16 v[84:87], v[148:151], v[132:135], v[84:87]\n\t" \
  "ds_read_b128 v[172:175], %[pa] offset:8960\n\t" \
  "v_mfma_f32_16x16x32_bf16 v[88:91], v[148:151], v[136:139], v[88:91]\n\t" \
  "ds_read_b64_tr_b16 v[180:181], %[ta1] offset:8192\n\t" \
  "v_mfma_f32_16x16x32_bf16 v[92:95], v[148:151], v[140:143], v[92:95]\n\t" \
  "ds_read_b64_tr_b16 v[182:183], %[ta1] offset:12288\n\t" \
  "v_mfma_f32_16x16x32_bf16 v[96:99], v[152:155], v[128:131], v[96:99]\n\t" \
  "ds_read_b64_tr_b16 v[184:185], %[ta2] offset:8192\n\t" \
  "v_mfma_f32_16x16x32_bf16 v[100:103], v[152:155], v[132:135], v[100:103]\n\t" \
  "ds_read_b64_tr_b16 v[186:187], %[ta2] offset:12288\n\t" \
  "v_mfma_f32_16x16x32_bf16 v[104:107], v[152:155], v[136:139], v[104:107]\n\t" \
  "ds_read_b64_tr_b16 v[188:189], %[ta3] offset:8192\n\t" \
  "v_mfma_f32_16x16x32_bf16 v[108:111], v[152:155], v[140:143], v[108:111]\n\t" \
  "ds_read_b64_tr_b16 v[190:191], %[ta3] offset:12288\n\t" \
  "v_mfma_f32_16x16x32_bf16 v[112:115], v[156:159], v[128:131], v[112:115]\n\t" \
  "v_mfma_f32_16x16x32_bf16 v[116:119], v[156:159], v[132:135], v[116:119]\n\t" \
  "v_mfma_f32_16x16x32_bf16 v[120:123], v[156:159], v[136:139], v[120:123]\n\t" \
  "v_mfma_f32_16x16x32_bf16 v[124:127], v[156:159], v[140:143], v[124:127]\n\t" \
  "s_waitcnt lgkmcnt(0)\n\t" \
  "s_barrier\n\t" \
  "v_mfma_f32_16x16x32_bf16 v[64:67], v[176:179], v[160:163], v[64:67]\n\t" \
  "ds_read_b64_tr_b16 v[144:145], %[ta0] offset:16384\n\t" \
  "v_mfma_f32_16x16x32_bf16 v[68:71], v[176:179], v[164:167], v[68:71]\n\t" \
  "ds_read_b64_tr_b16 v[146:147], %[ta0] offset:20480\n\t" \
  "v_mfma_f32_16x16x32_bf16 v[72:75], v[176:179], v[168:171], v[72:75]\n\t" \
  "ds_read_b128 v[128:131], %[pa] offset:16384\n\t" \
  "v_mfma_f32_16x16x32_bf16 v[76:79], v[176:179], v[172:175], v[76:79]\n\t" \
  "ds_read_b128 v[132:135], %[pa] offset:16640\n\t" \
  "v_mfma_f32_16x16x32_bf16 v[80:83], v[180:183], v[160:163], v[80:83]\n\t" \
  "ds_read_b128 v[136:139], %[pa] offset:16896\n\t" \
  "v_mfma_f32_16x16x32_bf16 v[84:87], v[180:183], v[164:167], v[84:87]\n\t" \
  "ds_read_b128 v[140:143], %[pa] offset:17152\n\t" \
  "v_mfma_f32_16x16x32_bf16 v[88:91], v[180:183], v[168:171], v[88:91]\n\t" \
  "ds_read_b64_tr_b16 v[148:149], %[ta1] offset:16384\n\t" \
  "v_mfma_f32_16x16x32_bf16 v[92:95], v[180:183], v[172:175], v[92:95]\n\t" \
  "ds_read_b64_tr_b16 v[150:151], %[ta1] offset:20480\n\t" \
  "v_mfma_f32_16x16x32_bf16 v[96:99], v[184:187], v[160:163], v[96:99]\n\t" \
  "ds_read_b64_tr_b16 v[152:153], %[ta2] offset:16384\n\t" \
  "v_mfma_f32_16x16x32_bf16 v[100:103], v[184:187], v[164:167], v[100:103]\n\t" \
  "ds_read_b64_tr_b16 v[154:155], %[ta2] offset:20480\n\t" \
  "v_mfma_f32_16x16x32_bf16 v[104:107], v[184:187], v[168:171], v[104:107]\n\t" \
  "ds_read_b64_tr_b16 v[156:157], %[ta3] offset:16384\n\t" \
  "v_mfma_f32_16x16x32_bf16 v[108:111], v[184:187], v[172:175], v[108:111]\n\t" \
  "ds_read_b64_tr_b16 v[158:159], %[ta3] offset:20480\n\t" \
  "v_mfma_f32_16x16x32_bf16 v[112:115], v[188:191], v[160:163], v[112:115]\n\t" \
  "v_mfma_f32_16x16x32_bf16 v[116:119], v[188:191], v[164:167], v[116:119]\n\t" \
  "v_mfma_f32_16x16x32_bf16 v[120:123], v[188:191], v[168:171], v[120:123]\n\t" \
  "v_mfma_f32_16x16x32_bf16 v[124:127], v[188:191], v[172:175], v[124:127]\n\t" \
  "s_waitcnt lgkmcnt(0)\n\t" \
  "v_mfma_f32_16x16x32_bf16 v[64:67], v[144:147], v[128:131], v[64:67]\n\t" \
  "ds_read_b64_tr_b16 v[176:177], %[ta0] offset:24576\n\t" \
  "v_mfma_f32_16x16x32_bf16 v[68:71], v[144:147], v[132:135], v[68:71]\n\t" \
  "ds_read_b64_tr_b16 v[178:179], %[ta0] offset:28672\n\t" \
  "v_mfma_f32_16x16x32_bf16 v[72:75], v[144:147], v[136:139], v[72:75]\n\t" \
  "ds_read_b128 v[160:163], %[pa] offset:24576\n\t" \
  "v_mfma_f32_16x16x32_bf16 v[76:79], v[144:147], v[140:143], v[76:79]\n\t" \
  "ds_read_b128 v[164:167], %[pa] offset:24832\n\t" \
  "v_mfma_f32_16x16x32_bf16 v[80:83], v[148:151], v[128:131], v[80:83]\n\t" \
  "ds_read_b128 v[168:171], %[pa] offset:25088\n\t" \
  "v_mfma_f32_16x16x32_bf16 v[84:87], v[148:151], v[132:135], v[84:87]\n\t" \
  "ds_read_b128 v[172:175], %[pa] offset:25344\n\t" \
  "v_mfma_f32_16x16x32_bf16 v[88:91], v[148:151], v[136:139], v[88:91]\n\t" \
  "ds_read_b64_tr_b16 v[180:181], %[ta1] offset:24576\n\t" \
  "v_mfma_f32_16x16x32_bf16 v[92:95], v[148:151], v[140:143], v[92:95]\n\t" \
  "ds_read_b64_tr_b16 v[182:183], %[ta1] offset:28672\n\t" \
  "v_mfma_f32_16x16x32_bf16 v[96:99], v[152:155], v[128:131], v[96:99]\n\t" \
  "ds_read_b64_tr_b16 v[184:185], %[ta2] offset:24576\n\t" \
  "v_mfma_f32_16x16x32_bf16 v[100:103], v[152:155], v[132:135], v[100:103]\n\t" \
  "ds_read_b64_tr_b16 v[186:187], %[ta2] offset:28672\n\t" \
  "v_mfma_f32_16x16x32_bf16 v[104:107], v[152:155], v[136:139], v[104:107]\n\t" \
  "ds_read_b64_tr_b16 v[188:189], %[ta3] offset:24576\n\t" \
  "v_mfma_f32_16x16x32_bf16 v[108:111], v[152:155], v[140:143], v[108:111]\n\t" \
  "ds_read_b64_tr_b16 v[190:191], %[ta3] offset:28672\n\t" \
  "v_mfma_f32_16x16x32_bf16 v[112:115], v[156:159], v[128:131], v[112:115]\n\t" \
  "v_mfma_f32_16x16x32_bf16 v[116:119], v[156:159], v[132:135], v[116:119]\n\t" \
  "v_mfma_f32_16x16x32_bf16 v[120:123], v[156:159], v[136:139], v[120:123]\n\t" \
  "v_mfma_f32_16x16x32_bf16 v[124:127], v[156:159], v[140:143], v[124:127]\n\t" \
  "s_waitcnt lgkmcnt(0)\n\t" \
  "s_barrier\n\t" \
  "v_mfma_f32_16x16x32_bf16 v[64:67], v[176:179], v[160:163], v[64:67]\n\t" \
  "ds_read_b64_tr_b16 v[144:145], %[ta0] offset:32768\n\t" \
  "v_mfma_f32_16x16x32_bf16 v[68:71], v[176:179], v[164:167], v[68:71]\n\t" \
  "ds_read_b64_tr_b16 v[146:147], %[ta0] offset:36864\n\t" \
  "v_mfma_f32_16x16x32_bf16 v[72:75], v[176:179], v[168:171], v[72:75]\n\t" \
  "ds_read_b128 v[128:131], %[pa] offset:32768\n\t" \
  "v_mfma_f32_16x16x32_bf16 v[76:79], v[176:179], v[172:175], v[76:79]\n\t" \
  "ds_read_b128 v[132:135], %[pa] offset:33024\n\t" \
  "v_mfma_f32_16x16x32_bf16 v[80:83], v[180:183], v[160:163], v[80:83]\n\t" \
  "ds_read_b128 v[136:139], %[pa] offset:33280\n\t" \
  "v_mfma_f32_16x16x32_bf16 v[84:87], v[180:183], v[164:167], v[84:87]\n\t" \
  "ds_read_b128 v[140:143], %[pa] offset:33536\n\t" \
  "v_mfma_f32_16x16x32_bf16 v[88:91], v[180:183], v[168:171], v[88:91]\n\t" \
  "ds_read_b64_tr_b16 v[148:149], %[ta1] offset:32768\n\t" \
  "v_mfma_f32_16x16x32_bf16 v[92:95], v[180:183], v[172:175], v[92:95]\n\t" \
  "ds_read_b64_tr_b16 v[150:151], %[ta1] offset:36864\n\t" \
  "v_mfma_f32_16x16x32_bf16 v[96:99], v[184:187], v[160:163], v[96:99]\n\t" \
  "ds_read_b64_tr_b16 v[152:153], %[ta2] offset:32768\n\t" \
  "v_mfma_f32_16x16x32_bf16 v[100:103], v[184:187], v[164:167], v[100:103]\n\t" \
  "ds_read_b64_tr_b16 v[154:155], %[ta2] offset:36864\n\t" \
  "v_mfma_f32_16x16x32_bf16 v[104:107], v[184:187], v[168:171], v[104:107]\n\t" \
  "ds_read_b64_tr_b16 v[156:157], %[ta3] offset:32768\n\t" \
  "v_mfma_f32_16x16x32_bf16 v[108:111], v[184:187], v[172:175], v[108:111]\n\t" \
  "ds_read_b64_tr_b16 v[158:159], %[ta3] offset:36864\n\t" \
  "v_mfma_f32_16x16x32_bf16 v[112:115], v[188:191], v[160:163], v[112:115]\n\t" \
  "v_mfma_f32_16x16x32_bf16 v[116:119], v[188:191], v[164:167], v[116:119]\n\t" \
  "v_mfma_f32_16x16x32_bf16 v[120:123], v[188:191], v[168:171], v[120:123]\n\t" \
  "v_mfma_f32_16x16x32_bf16 v[124:127], v[188:191], v[172:175], v[124:127]\n\t" \
  "s_waitcnt lgkmcnt(0)\n\t" \
  "v_mfma_f32_16x16x32_bf16 v[64:67], v[144:147], v[128:131], v[64:67]\n\t" \
  "ds_read_b64_tr_b16 v[176:177], %[ta0] offset:40960\n\t" \
  "v_mfma_f32_16x16x32_bf16 v[68:71], v[144:147], v[132:135], v[68:71]\n\t" \
  "ds_read_b64_tr_b16 v[178:179], %[ta0] offset:45056\n\t" \
  "v_mfma_f32_16x16x32_bf16 v[72:75], v[144:147], v[136:139], v[72:75]\n\t" \
  "ds_read_b128 v[160:163], %[pa] offset:40960\n\t" \
  "v_mfma_f32_16x16x32_bf16 v[76:79], v[144:147], v[140:143], v[76:79]\n\t" \
  "ds_read_b128 v[164:167], %[pa] offset:41216\n\t" \
  "v_mfma_f32_16x16x32_bf16 v[80:83], v[148:151], v[128:131], v[80:83]\n\t" \
  "ds_read_b128 v[168:171], %[pa] offset:41472\n\t" \
  "v_mfma_f32_16x16x32_bf16 v[84:87], v[148:151], v[132:135], v[84:87]\n\t" \
  "ds_read_b128 v[172:175], %[pa] offset:41728\n\t" \
  "v_mfma_f32_16x16x32_bf16 v[88:91], v[148:151], v[136:139], v[88:91]\n\t" \
  "ds_read_b64_tr_b16 v[180:181], %[ta1] offset:40960\n\t" \
  "v_mfma_f32_16x16x32_bf16 v[92:95], v[148:151], v[140:143], v[92:95]\n\t" \
  "ds_read_b64_tr_b16 v[182:183], %[ta1] offset:45056\n\t" \
  "v_mfma_f32_16x16x32_bf16 v[96:99], v[152:155], v[128:131], v[96:99]\n\t" \
  "ds_read_b64_tr_b16 v[184:185], %[ta2] offset:40960\n\t" \
  "v_mfma_f32_16x16x32_bf16 v[100:103], v[152:155], v[132:135], v[100:103]\n\t" \
  "ds_read_b64_tr_b16 v[186:187], %[ta2] offset:45056\n\t" \
  "v_mfma_f32_16x16x32_bf16 v[104:107], v[152:155], v[136:139], v[104:107]\n\t" \
  "ds_read_b64_tr_b16 v[188:189], %[ta3] offset:40960\n\t" \
  "v_mfma_f32_16x16x32_bf16 v[108:111], v[152:155], v[140:143], v[108:111]\n\t" \
  "ds_read_b64_tr_b16 v[190:191], %[ta3] offset:45056\n\t" \
  "v_mfma_f32_16x16x32_bf16 v[112:115], v[156:159], v[128:131], v[112:115]\n\t" \
  "v_mfma_f32_16x16x32_bf16 v[116:119], v[156:159], v[132:135], v[116:119]\n\t" \
  "v_mfma_f32_16x16x32_bf16 v[120:123], v[156:159], v[136:139], v[120:123]\n\t" \
  "v_mfma_f32_16x16x32_bf16 v[124:127], v[156:159], v[140:143], v[124:127]\n\t" \
  "s_waitcnt lgkmcnt(0)\n\t" \
  "s_barrier\n\t" \
  "v_mfma_f32_16x16x32_bf16 v[64:67], v[176:179], v[160:163], v[64:67]\n\t" \
  "ds_read_b64_tr_b16 v[144:145], %[ta0] offset:49152\n\t" \
  "v_mfma_f32_16x16x32_bf16 v[68:71], v[176:179], v[164:167], v[68:71]\n\t" \
  "ds_read_b64_tr_b16 v[146:147], %[ta0] offset:53248\n\t" \
  "v_mfma_f32_16x16x32_bf16 v[72:75], v[176:179], v[168:171], v[72:75]\n\t" \
  "ds_read_b128 v[128:131], %[pa] offset:49152\n\t" \
  "v_mfma_f32_16x16x32_bf16 v[76:79], v[176:179], v[172:175], v[76:79]\n\t" \
  "ds_read_b128 v[132:135], %[pa] offset:49408\n\t" \
  "v_mfma_f32_16x16x32_bf16 v[80:83], v[180:183], v[160:163], v[80:83]\n\t" \
  "ds_read_b128 v[136:139], %[pa] offset:49664\n\t" \
  "v_mfma_f32_16x16x32_bf16 v[84:87], v[180:183], v[164:167], v[84:87]\n\t" \
  "ds_read_b128 v[140:143], %[pa] offset:49920\n\t" \
  "v_mfma_f32_16x16x32_bf16 v[88:91], v[180:183], v[168:171], v[88:91]\n\t" \
  "ds_read_b64_tr_b16 v[148:149], %[ta1] offset:49152\n\t" \
  "v_mfma_f32_16x16x32_bf16 v[92:95], v[180:183], v[172:175], v[92:95]\n\t" \
  "ds_read_b64_tr_b16 v[150:151], %[ta1] offset:53248\n\t" \
  "v_mfma_f32_16x16x32_bf16 v[96:99], v[184:187], v[160:163], v[96:99]\n\t" \
  "ds_read_b64_tr_b16 v[152:153], %[ta2] offset:49152\n\t" \
  "v_mfma_f32_16x16x32_bf16 v[100:103], v[184:187], v[164:167], v[100:103]\n\t" \
  "ds_read_b64_tr_b16 v[154:155], %[ta2] offset:53248\n\t" \
  "v_mfma_f32_16x16x32_bf16 v[104:107], v[184:187], v[168:171], v[104:107]\n\t" \
  "ds_read_b64_tr_b16 v[156:157], %[ta3] offset:49152\n\t" \
  "v_mfma_f32_16x16x32_bf16 v[108:111], v[184:187], v[172:175], v[108:111]\n\t" \
  "ds_read_b64_tr_b16 v[158:159], %[ta3] offset:53248\n\t" \
  "v_mfma_f32_16x16x32_bf16 v[112:115], v[188:191], v[160:163], v[112:115]\n\t" \
  "v_mfma_f32_16x16x32_bf16 v[116:119], v[188:191], v[164:167], v[116:119]\n\t" \
  "v_mfma_f32_16x16x32_bf16 v[120:123], v[188:191], v[168:171], v[120:123]\n\t" \
  "v_mfma_f32_16x16x32_bf16 v[124:127], v[188:191], v[172:175], v[124:127]\n\t" \
  "s_waitcnt lgkmcnt(0)\n\t" \
  "v_mfma_f32_16x16x32_bf16 v[64:67], v[144:147], v[128:131], v[64:67]\n\t" \
  "ds_read_b64_tr_b16 v[176:177], %[ta0] offset:57344\n\t" \
  "v_mfma_f32_16x16x32_bf16 v[68:71], v[144:147], v[132:135], v[68:71]\n\t" \
  "ds_read_b64_tr_b16 v[178:179], %[ta0] offset:61440\n\t" \
  "v_mfma_f32_16x16x32_bf16 v[72:75], v[144:147], v[136:139], v[72:75]\n\t" \
  "ds_read_b128 v[160:163], %[pa] offset:57344\n\t" \
  "v_mfma_f32_16x16x32_bf16 v[76:79], v[144:147], v[140:143], v[76:79]\n\t" \
  "ds_read_b128 v[164:167], %[pa] offset:57600\n\t" \
  "v_mfma_f32_16x16x32_bf16 v[80:83], v[148:151], v[128:131], v[80:83]\n\t" \
  "ds_read_b128 v[168:171], %[pa] offset:57856\n\t" \
  "v_mfma_f32_16x16x32_bf16 v[84:87], v[148:151], v[132:135], v[84:87]\n\t" \
  "ds_read_b128 v[172:175], %[pa] offset:58112\n\t" \
  "v_mfma_f32_16x16x32_bf16 v[88:91], v[148:151], v[136:139], v[88:91]\n\t" \
  "ds_read_b64_tr_b16 v[180:181], %[ta1] offset:57344\n\t" \
  "v_mfma_f32_16x16x32_bf16 v[92:95], v[148:151], v[140:143], v[92:95]\n\t" \
  "ds_read_b64_tr_b16 v[182:183], %[ta1] offset:61440\n\t" \
  "v_mfma_f32_16x16x32_bf16 v[96:99], v[152:155], v[128:131], v[96:99]\n\t" \
  "ds_read_b64_tr_b16 v[184:185], %[ta2] offset:57344\n\t" \
  "v_mfma_f32_16x16x32_bf16 v[100:103], v[152:155], v[132:135], v[100:103]\n\t" \
  "ds_read_b64_tr_b16 v[186:187], %[ta2] offset:61440\n\t" \
  "v_mfma_f32_16x16x32_bf16 v[104:107], v[152:155], v[136:139], v[104:107]\n\t" \
  "ds_read_b64_tr_b16 v[188:189], %[ta3] offset:57344\n\t" \
  "v_mfma_f32_16x16x32_bf16 v[108:111], v[152:155], v[140:143], v[108:111]\n\t" \
  "ds_read_b64_tr_b16 v[190:191], %[ta3] offset:61440\n\t" \
  "v_mfma_f32_16x16x32_bf16 v[112:115], v[156:159], v[128:131], v[112:115]\n\t" \
  "v_mfma_f32_16x16x32_bf16 v[116:119], v[156:159], v[132:135], v[116:119]\n\t" \
  "v_mfma_f32_16x16x32_bf16 v[120:123], v[156:159], v[136:139], v[120:123]\n\t" \
  "v_mfma_f32_16x16x32_bf16 v[124:127], v[156:159], v[140:143], v[124:127]\n\t" \
  "s_sub_u32 %[cnt], %[cnt], 1\n\t" \
  "s_cmp_eq_u32 %[cnt], 0\n\t" \
  "s_cbranch_scc1 2f\n\t" \
  "s_waitcnt lgkmcnt(0)\n\t" \
  "s_barrier\n\t" \
  "v_mfma_f32_16x16x32_bf16 v[64:67], v[176:179], v[160:163], v[64:67]\n\t" \
  "ds_read_b64_tr_b16 v[144:145], %[ta0] offset:0\n\t" \
  "v_mfma_f32_16x16x32_bf16 v[68:71], v[176:179], v[164:167], v[68:71]\n\t" \
  "ds_read_b64_tr_b16 v[146:147], %[ta0] offset:4096\n\t" \
  "v_mfma_f32_16x16x32_bf16 v[72:75], v[176:179], v[168:171], v[72:75]\n\t" \
  "ds_read_b128 v[128:131], %[pa] offset:0\n\t" \
  "v_mfma_f32_16x16x32_bf16 v[76:79], v[176:179], v[172:175], v[76:79]\n\t" \
  "ds_read_b128 v[132:135], %[pa] offset:256\n\t" \
  "v_mfma_f32_16x16x32_bf16 v[80:83], v[180:183], v[160:163], v[80:83]\n\t" \
  "ds_read_b128 v[136:139], %[pa] offset:512\n\t" \
  "v_mfma_f32_16x16x32_bf16 v[84:87], v[180:183], v[164:167], v[84:87]\n\t" \
  "ds_read_b128 v[140:143], %[pa] offset:768\n\t" \
  "v_mfma_f32_16x16x32_bf16 v[88:91], v[180:183], v[168:171], v[88:91]\n\t" \
  "ds_read_b64_tr_b16 v[148:149], %[ta1] offset:0\n\t" \
  "v_mfma_f32_16x16x32_bf16 v[92:95], v[180:183], v[172:175], v[92:95]\n\t" \
  "ds_read_b64_tr_b16 v[150:151], %[ta1] offset:4096\n\t" \
  "v_mfma_f32_16x16x32_bf16 v[96:99], v[184:187], v[160:163], v[96:99]\n\t" \
  "ds_read_b64_tr_b16 v[152:153], %[ta2] offset:0\n\t" \
  "v_mfma_f32_16x16x32_bf16 v[100:103], v[184:187], v[164:167], v[100:103]\n\t" \
  "ds_read_b64_tr_b16 v[154:155], %[ta2] offset:4096\n\t" \
  "v_mfma_f32_16x16x32_bf16 v[104:107], v[184:187], v[168:171], v[104:107]\n\t" \
  "ds_read_b64_tr_b16 v[156:157], %[ta3] offset:0\n\t" \
  "v_mfma_f32_16x16x32_bf16 v[108:111], v[184:187], v[172:175], v[108:111]\n\t" \
  "ds_read_b64_tr_b16 v[158:159], %[ta3] offset:4096\n\t" \
  "v_mfma_f32_16x16x32_bf16 v[112:115], v[188:191], v[160:163], v[112:115]\n\t" \
  "v_mfma_f32_16x16x32_bf16 v[116:119], v[188:191], v[164:167], v[116:119]\n\t" \
  "v_mfma_f32_16x16x32_bf16 v[120:123], v[188:191], v[168:171], v[120:123]\n\t" \
  "v_mfma_f32_16x16x32_bf16 v[124:127], v[188:191], v[172:175], v[124:127]\n\t" \
  "s_branch 1b\n\t" \
  "2:\n\t" \
  "s_waitcnt lgkmcnt(0)\n\t" \
  "v_mfma_f32_16x16x32_bf16 v[64:67], v[176:179], v[160:163], v[64:67]\n\t" \
  "v_mfma_f32_16x16x32_bf16 v[68:71], v[176:179], v[164:167], v[68:71]\n\t" \
  "v_mfma_f32_16x16x32_bf16 v[72:75], v[176:179], v[168:171], v[72:75]\n\t" \
  "v_mfma_f32_16x16x32_bf16 v[76:79], v[176:179], v[172:175], v[76:79]\n\t" \
  "v_mfma_f32_16x16x32_bf16 v[80:83], v[180:183], v[160:163], v[80:83]\n\t" \
  "v_mfma_f32_16x16x32_bf16 v[84:87], v[180:183], v[164:167], v[84:87]\n\t" \
  "v_mfma_f32_16x16x32_bf16 v[88:91], v[180:183], v[168:171], v[88:91]\n\t" \
  "v_mfma_f32_16x16x32_bf16 v[92:95], v[180:183], v[172:175], v[92:95]\n\t" \
  "v_mfma_f32_16x16x32_bf16 v[96:99], v[184:187], v[160:163], v[96:99]\n\t" \
  "v_mfma_f32_16x16x32_bf16 v[100:103], v[184:187], v[164:167], v[100:103]\n\t" \
  "v_mfma_f32_16x16x32_bf16 v[104:107], v[184:187], v[168:171], v[104:107]\n\t" \
  "v_mfma_f32_16x16x32_bf16 v[108:111], v[184:187], v[172:175], v[108:111]\n\t" \
  "v_mfma_f32_16x16x32_bf16 v[112:115], v[188:191], v[160:163], v[112:115]\n\t" \
  "v_mfma_f32_16x16x32_bf16 v[116:119], v[188:191], v[164:167], v[116:119]\n\t" \
  "v_mfma_f32_16x16x32_bf16 v[120:123], v[188:191], v[168:171], v[120:123]\n\t" \
  "v_mfma_f32_16x16x32_bf16 v[124:127], v[188:191], v[172:175], v[124:127]\n\t" \
  "s_nop 15\n\t"
#define KLOOP_TS \
  "ds_read_b64_tr_b16 v[144:145], %[ta0] offset:0\n\t" \
  "ds_read_b64_tr_b16 v[146:147], %[ta0] offset:4096\n\t" \
  "ds_read_b128 v[128:131], %[pa] offset:0\n\t" \
  "ds_read_b128 v[132:135], %[pa] offset:256\n\t" \
  "ds_read_b128 v[136:139], %[pa] offset:512\n\t" \
  "ds_read_b128 v[140:143], %[pa] offset:768\n\t" \
  "ds_read_b64_tr_b16 v[148:149], %[ta1] offset:0\n\t" \
  "ds_read_b64_tr_b16 v[150:151], %[ta1] offset:4096\n\t" \
  "ds_read_b64_tr_b16 v[152:153], %[ta2] offset:0\n\t" \
  "ds_read_b64_tr_b16 v[154:155], %[ta2] offset:4096\n\t" \
  "ds_read_b64_tr_b16 v[156:157], %[ta3] offset:0\n\t" \
  "ds_read_b64_tr_b16 v[158:159], %[ta3] offset:4096\n\t" \
  "s_mov_b32 %[cnt], %[nq]\n\t" \
  ".p2align 6\n\t" \
  "1:\n\t" \
  "s_waitcnt lgkmcnt(0)\n\t" \
  "v_mfma_f32_16x16x32_bf16 v[64:67], v[144:147], v[128:131], v[64:67]\n\t" \
  "ds_read_b64_tr_b16 v[176:177], %[ta0] offset:8192\n\t" \
  "v_mfma_f32_16x16x32_bf16 v[68:71], v[144:147], v[132:135], v[68:71]\n\t" \
  "ds_read_b64_tr_b16 v[178:179], %[ta0] offset:12288\n\t" \
  "v_mfma_f32_16x16x32_bf16 v[72:75], v[144:147], v[136:139], v[72:75]\n\t" \
  "ds_read_b128 v[160:163], %[pa] offset:8192\n\t" \
  "v_mfma_f32_16x16x32_bf16 v[76:79], v[144:147], v[140:143], v[76:79]\n\t" \
  "ds_read_b128 v[164:167], %[pa] offset:8448\n\t" \
  "v_mfma_f32_16x16x32_bf16 v[80:83], v[148:151], v[128:131], v[80:83]\n\t" \
  "ds_read_b128 v[168:171], %[pa] offset:8704\n\t" \
  "v_mfma_f32_16x16x32_bf16 v[84:87], v[148:151], v[132:135], v[84:87]\n\t" \
  "ds_read_b128 v[172:175], %[pa] offset:8960\n\t" \
  "v_mfma_f32_16x16x32_bf16 v[88:91], v[148:151], v[136:139], v[88:91]\n\t" \
  "ds_read_b64_tr_b16 v[180:181], %[ta1] offset:8192\n\t" \
  "v_mfma_f32_16x16x32_bf16 v[92:95], v[148:151], v[140:143], v[92:95]\n\t" \
  "ds_read_b64_tr_b16 v[182:183], %[ta1] offset:12288\n\t" \
  "v_mfma_f32_16x16x32_bf16 v[96:99], v[152:155], v[128:131], v[96:99]\n\t" \
  "ds_read_b64_tr_b16 v[184:185], %[ta2] offset:8192\n\t" \
  "v_mfma_f32_16x16x32_bf16 v[100:103], v[152:155], v[132:135], v[100:103]\n\t" \
  "ds_read_b64_tr_b16 v[186:187], %[ta2] offset:12288\n\t" \
  "v_mfma_f32_16x16x32_bf16 v[104:107], v[152:155], v[136:139], v[104:107]\n\t" \
  "ds_read_b64_tr_b16 v[188:189], %[ta3] offset:8192\n\t" \
  "v_mfma_f32_16x16x32_bf16 v[108:111], v[152:155], v[140:143], v[108:111]\n\t" \
  "ds_read_b64_tr_b16 v[190:191], %[ta3] offset:12288\n\t" \
  "v_mfma_f32_16x16x32_bf16 v[112:115], v[156:159], v[128:131], v[112:115]\n\t" \
  "v_mfma_f32_16x16x32_bf16 v[116:119], v[156:159], v[132:135], v[116:119]\n\t" \
  "v_mfma_f32_16x16x32_bf16 v[120:123], v[156:159], v[136:139], v[120:123]\n\t" \
  "v_mfma_f32_16x16x32_bf16 v[124:127], v[156:159], v[140:143], v[124:127]\n\t" \
  "s_waitcnt lgkmcnt(0)\n\t" \
  "s_barrier\n\t" \
  "v_mfma_f32_16x16x32_bf16 v[64:67], v[176:179], v[160:163], v[64:67]\n\t" \
  "ds_read_b64_tr_b16 v[144:145], %[ta0] offset:16384\n\t" \
  "v_mfma_f32_16x16x32_bf16 v[68:71], v[176:179], v[164:167], v[68:71]\n\t" \
  "ds_read_b64_tr_b16 v[146:147], %[ta0] offset:20480\n\t" \
  "v_mfma_f32_16x16x32_bf16 v[72:75], v[176:179], v[168:171], v[72:75]\n\t" \
  "ds_read_b128 v[128:131], %[pa] offset:16384\n\t" \
  "v_mfma_f32_16x16x32_bf16 v[76:79], v[176:179], v[172:175], v[76:79]\n\t" \
  "ds_read_b128 v[132:135], %[pa] offset:16640\n\t" \
  "v_mfma_f32_16x16x32_bf16 v[80:83], v[180:183], v[160:163], v[80:83]\n\t" \
  "ds_read_b128 v[136:139], %[pa] offset:16896\n\t" \
  "v_mfma_f32_16x16x32_bf16 v[84:87], v[180:183], v[164:167], v[84:87]\n\t" \
  "ds_read_b128 v[140:143], %[pa] offset:17152\n\t" \
  "v_mfma_f32_16x16x32_bf16 v[88:91], v[180:183], v[168:171], v[88:91]\n\t" \
  "ds_read_b64_tr_b16 v[148:149], %[ta1] offset:16384\n\t" \
  "v_mfma_f32_16x16x32_bf16 v[92:95], v[180:183], v[172:175], v[92:95]\n\t" \
  "ds_read_b64_tr_b16 v[150:151], %[ta1] offset:20480\n\t" \
  "v_mfma_f32_16x16x32_bf16 v[96:99], v[184:187], v[160:163], v[96:99]\n\t" \
  "ds_read_b64_tr_b16 v[152:153], %[ta2] offset:16384\n\t" \
  "v_mfma_f32_16x16x32_bf16 v[100:103], v[184:187], v[164:167], v[100:103]\n\t" \
  "ds_read_b64_tr_b16 v[154:155], %[ta2] offset:20480\n\t" \
  "v_mfma_f32_16x16x32_bf16 v[104:107], v[184:187], v[168:171], v[104:107]\n\t" \
  "ds_read_b64_tr_b16 v[156:157], %[ta3] offset:16384\n\t" \
  "v_mfma_f32_16x16x32_bf16 v[108:111], v[184:187], v[172:175], v[108:111]\n\t" \
  "ds_read_b64_tr_b16 v[158:159], %[ta3] offset:20480\n\t" \
  "v_mfma_f32_16x16x32_bf16 v[112:115], v[188:191], v[160:163], v[112:115]\n\t" \
  "v_mfma_f32_16x16x32_bf16 v[116:119], v[188:191], v[164:167], v[116:119]\n\t" \
  "v_mfma_f32_16x16x32_bf16 v[120:123], v[188:191], v[168:171], v[120:123]\n\t" \
  "v_mfma_f32_16x16x32_bf16 v[124:127], v[188:191], v[172:175], v[124:127]\n\t" \
  "s_waitcnt lgkmcnt(0)\n\t" \
  "v_mfma_f32_16x16x32_bf16 v[64:67], v[144:147], v[128:131], v[64:67]\n\t" \
  "ds_read_b64_tr_b16 v[176:177], %[ta0] offset:24576\n\t" \
  "v_mfma_f32_16x16x32_bf16 v[68:71], v[144:147], v[132:135], v[68:71]\n\t" \
  "ds_read_b64_tr_b16 v[178:179], %[ta0] offset:28672\n\t" \
  "v_mfma_f32_16x16x32_bf16 v[72:75], v[144:147], v[136:139], v[72:75]\n\t" \
  "ds_read_b128 v[160:163], %[pa] offset:24576\n\t" \
  "v_mfma_f32_16x16x32_bf16 v[76:79], v[144:147], v[140:143], v[76:79]\n\t" \
  "ds_read_b128 v[164:167], %[pa] offset:24832\n\t" \
  "v_mfma_f32_16x16x32_bf16 v[80:83], v[148:151], v[128:131], v[80:83]\n\t" \
  "ds_read_b128 v[168:171], %[pa] offset:25088\n\t" \
  "v_mfma_f32_16x16x32_bf16 v[84:87], v[148:151], v[132:135], v[84:87]\n\t" \
  "ds_read_b128 v[172:175], %[pa] offset:25344\n\t" \
  "v_mfma_f32_16x16x32_bf16 v[88:91], v[148:151], v[136:139], v[88:91]\n\t" \
  "ds_read_b64_tr_b16 v[180:181], %[ta1] offset:24576\n\t" \
  "v_mfma_f32_16x16x32_bf16 v[92:95], v[148:151], v[140:143], v[92:95]\n\t" \
  "ds_read_b64_tr_b16 v[182:183], %[ta1] offset:28672\n\t" \
  "v_mfma_f32_16x16x32_bf16 v[96:99], v[152:155], v[128:131], v[96:99]\n\t" \
  "ds_read_b64_tr_b16 v[184:185], %[ta2] offset:24576\n\t" \
  "v_mfma_f32_16x16x32_bf16 v[100:103], v[152:155], v[132:135], v[100:103]\n\t" \
  "ds_read_b64_tr_b16 v[186:187], %[ta2] offset:28672\n\t" \
  "v_mfma_f32_16x16x32_bf16 v[104:107], v[152:155], v[136:139], v[104:107]\n\t" \
  "ds_read_b64_tr_b16 v[188:189], %[ta3] offset:24576\n\t" \
  "v_mfma_f32_16x16x32_bf16 v[108:111], v[152:155], v[140:143], v[108:111]\n\t" \
  "ds_read_b64_tr_b16 v[190:191], %[ta3] offset:28672\n\t" \
  "v_mfma_f32_16x16x32_bf16 v[112:115], v[156:159], v[128:131], v[112:115]\n\t" \
  "v_mfma_f32_16x16x32_bf16 v[116:119], v[156:159], v[132:135], v[116:119]\n\t" \
  "v_mfma_f32_16x16x32_bf16 v[120:123], v[156:159], v[136:139], v[120:123]\n\t" \
  "v_mfma_f32_16x16x32_bf16 v[124:127], v[156:159], v[140:143], v[124:127]\n\t" \
  "s_waitcnt lgkmcnt(0)\n\t" \
  "s_barrier\n\t" \
  "v_mfma_f32_16x16x32_bf16 v[64:67], v[176:179], v[160:163], v[64:67]\n\t" \
  "ds_read_b64_tr_b16 v[144:145], %[ta0] offset:32768\n\t" \
  "v_mfma_f32_16x16x32_bf16 v[68:71], v[176:179], v[164:167], v[68:71]\n\t" \
  "ds_read_b64_tr_b16 v[146:147], %[ta0] offset:36864\n\t" \
  "v_mfma_f32_16x16x32_bf16 v[72:75], v[176:179], v[168:171], v[72:75]\n\t" \
  "ds_read_b128 v[128:131], %[pa] offset:32768\n\t" \
  "v_mfma_f32_16x16x32_bf16 v[76:79], v[176:179], v[172:175], v[76:79]\n\t" \
  "ds_read_b128 v[132:135], %[pa] offset:33024\n\t" \
  "v_mfma_f32_16x16x32_bf16 v[80:83], v[180:183], v[160:163], v[80:83]\n\t" \
  "ds_read_b128 v[136:139], %[pa] offset:33280\n\t" \
  "v_mfma_f32_16x16x32_bf16 v[84:87], v[180:183], v[164:167], v[84:87]\n\t" \
  "ds_read_b128 v[140:143], %[pa] offset:33536\n\t" \
  "v_mfma_f32_16x16x32_bf16 v[88:91], v[180:183], v[168:171], v[88:91]\n\t" \
  "ds_read_b64_tr_b16 v[148:149], %[ta1] offset:32768\n\t" \
  "v_mfma_f32_16x16x32_bf16 v[92:95], v[180:183], v[172:175], v[92:95]\n\t" \
  "ds_read_b64_tr_b16 v[150:151], %[ta1] offset:36864\n\t" \
  "v_mfma_f32_16x16x32_bf16 v[96:99], v[184:187], v[160:163], v[96:99]\n\t" \
  "ds_read_b64_tr_b16 v[152:153], %[ta2] offset:32768\n\t" \
  "v_mfma_f32_16x16x32_bf16 v[100:103], v[184:187], v[164:167], v[100:103]\n\t" \
  "ds_read_b64_tr_b16 v[154:155], %[ta2] offset:36864\n\t" \
  "v_mfma_f32_16x16x32_bf16 v[104:107], v[184:187], v[168:171], v[104:107]\n\t" \
  "ds_read_b64_tr_b16 v[156:157], %[ta3] offset:32768\n\t" \
  "v_mfma_f32_16x16x32_bf16 v[108:111], v[184:187], v[172:175], v[108:111]\n\t" \
  "ds_read_b64_tr_b16 v[158:159], %[ta3] offset:36864\n\t" \
  "v_mfma_f32_16x16x32_bf16 v[112:115], v[188:191], v[160:163], v[112:115]\n\t" \
  "v_mfma_f32_16x16x32_bf16 v[116:119], v[188:191], v[164:167], v[116:119]\n\t" \
  "v_mfma_f32_16x16x32_bf16 v[120:123], v[188:191], v[168:171], v[120:123]\n\t" \
  "v_mfma_f32_16x16x32_bf16 v[124:127], v[188:191], v[172:175], v[124:127]\n\t" \
  "s_waitcnt lgkmcnt(0)\n\t" \
  "v_mfma_f32_16x16x32_bf16 v[64:67], v[144:147], v[128:131], v[64:67]\n\t" \
  "ds_read_b64_tr_b16 v[176:177], %[ta0] offset:40960\n\t" \
  "v_mfma_f32_16x16x32_bf16 v[68:71], v[144:147], v[132:135], v[68:71]\n\t" \
  "ds_read_b64_tr_b16 v[178:179], %[ta0] offset:45056\n\t" \
  "v_mfma_f32_16x16x32_bf16 v[72:75], v[144:147], v[136:139], v[72:75]\n\t" \
  "ds_read_b128 v[160:163], %[pa] offset:40960\n\t" \
  "v_mfma_f32_16x16x32_bf16 v[76:79], v[144:147], v[140:143], v[76:79]\n\t" \
  "ds_read_b128 v[164:167], %[pa] offset:41216\n\t" \
  "v_mfma_f32_16x16x32_bf16 v[80:83], v[148:151], v[128:131], v[80:83]\n\t" \
  "ds_read_b128 v[168:171], %[pa] offset:41472\n\t" \
  "v_mfma_f32_16x16x32_bf16 v[84:87], v[148:151], v[132:135], v[84:87]\n\t" \
  "ds_read_b128 v[172:175], %[pa] offset:41728\n\t" \
  "v_mfma_f32_16x16x32_bf16 v[88:91], v[148:151], v[136:139], v[88:91]\n\t" \
  "ds_read_b64_tr_b16 v[180:181], %[ta1] offset:40960\n\t" \
  "v_mfma_f32_16x16x32_bf16 v[92:95], v[148:151], v[140:143], v[92:95]\n\t" \
  "ds_read_b64_tr_b16 v[182:183], %[ta1] offset:45056\n\t" \
  "v_mfma_f32_16x16x32_bf16 v[96:99], v[152:155], v[128:131], v[96:99]\n\t" \
  "ds_read_b64_tr_b16 v[184:185], %[ta2] offset:40960\n\t" \
  "v_mfma_f32_16x16x32_bf16 v[100:103], v[152:155], v[132:135], v[100:103]\n\t" \
  "ds_read_b64_tr_b16 v[186:187], %[ta2] offset:45056\n\t" \
  "v_mfma_f32_16x16x32_bf16 v[104:107], v[152:155], v[136:139], v[104:107]\n\t" \
  "ds_read_b64_tr_b16 v[188:189], %[ta3] offset:40960\n\t" \
  "v_mfma_f32_16x16x32_bf16 v[108:111], v[152:155], v[140:143], v[108:111]\n\t" \
  "ds_read_b64_tr_b16 v[190:191], %[ta3] offset:45056\n\t" \
  "v_mfma_f32_16x16x32_bf16 v[112:115], v[156:159], v[128:131], v[112:115]\n\t" \
  "v_mfma_f32_16x16x32_bf16 v[116:119], v[156:159], v[132:135], v[116:119]\n\t" \
  "v_mfma_f32_16x16x32_bf16 v[120:123], v[156:159], v[136:139], v[120:123]\n\t" \
  "v_mfma_f32_16x16x32_bf16 v[124:127], v[156:159], v[140:143], v[124:127]\n\t" \
  "s_waitcnt lgkmcnt(0)\n\t" \
  "s_barrier\n\t" \
  "v_mfma_f32_16x16x32_bf16 v[64:67], v[176:179], v[160:163], v[64:67]\n\t" \
  "ds_read_b64_tr_b16 v[144:145], %[ta0] offset:49152\n\t" \
  "v_mfma_f32_16x16x32_bf16 v[68:71], v[176:179], v[164:167], v[68:71]\n\t" \
  "ds_read_b64_tr_b16 v[146:147], %[ta0] offset:53248\n\t" \
  "v_mfma_f32_16x16x32_bf16 v[72:75], v[176:179], v[168:171], v[72:75]\n\t" \
  "ds_read_b128 v[128:131], %[pa] offset:49152\n\t" \
  "v_mfma_f32_16x16x32_bf16 v[76:79], v[176:179], v[172:175], v[76:79]\n\t" \
  "ds_read_b128 v[132:135], %[pa] offset:49408\n\t" \
  "v_mfma_f32_16x16x32_bf16 v[80:83], v[180:183], v[160:163], v[80:83]\n\t" \
  "ds_read_b128 v[136:139], %[pa] offset:49664\n\t" \
  "v_mfma_f32_16x16x32_bf16 v[84:87], v[180:183], v[164:167], v[84:87]\n\t" \
  "ds_read_b128 v[140:143], %[pa] offset:49920\n\t" \
  "v_mfma_f32_16x16x32_bf16 v[88:91], v[180:183], v[168:171], v[88:91]\n\t" \
  "ds_read_b64_tr_b16 v[148:149], %[ta1] offset:49152\n\t" \
  "v_mfma_f32_16x16x32_bf16 v[92:95], v[180:183], v[172:175], v[92:95]\n\t" \
  "ds_read_b64_tr_b16 v[150:151], %[ta1] offset:53248\n\t" \
  "v_mfma_f32_16x16x32_bf16 v[96:99], v[184:187], v[160:163], v[96:99]\n\t" \
  "ds_read_b64_tr_b16 v[152:153], %[ta2] offset:49152\n\t" \
  "v_mfma_f32_16x16x32_bf16 v[100:103], v[184:187], v[164:167], v[100:103]\n\t" \
  "ds_read_b64_tr_b16 v[154:155], %[ta2] offset:53248\n\t" \
  "v_mfma_f32_16x16x32_bf16 v[104:107], v[184:187], v[168:171], v[104:107]\n\t" \
  "ds_read_b64_tr_b16 v[156:157], %[ta3] offset:49152\n\t" \
  "v_mfma_f32_16x16x32_bf16 v[108:111], v[184:187], v[172:175], v[108:111]\n\t" \
  "ds_read_b64_tr_b16 v[158:159], %[ta3] offset:53248\n\t" \
  "v_mfma_f32_16x16x32_bf16 v[112:115], v[188:191], v[160:163], v[112:115]\n\t" \
  "v_mfma_f32_16x16x32_bf16 v[116:119], v[188:191], v[164:167], v[116:119]\n\t" \
  "v_mfma_f32_16x16x32_bf16 v[120:123], v[188:191], v[168:171], v[120:123]\n\t" \
  "v_mfma_f32_16x16x32_bf16 v[124:127], v[188:191], v[172:175], v[124:127]\n\t" \
  "s_waitcnt lgkmcnt(0)\n\t" \
  "v_mfma_f32_16x16x32_bf16 v[64:67], v[144:147], v[128:131], v[64:67]\n\t" \
  "ds_read_b64_tr_b16 v[176:177], %[ta0] offset:57344\n\t" \
  "v_mfma_f32_16x16x32_bf16 v[68:71], v[144:147], v[132:135], v[68:71]\n\t" \
  "ds_read_b64_tr_b16 v[178:179], %[ta0] offset:61440\n\t" \
  "v_mfma_f32_16x16x32_bf16 v[72:75], v[144:147], v[136:139], v[72:75]\n\t" \
  "ds_read_b128 v[160:163], %[pa] offset:57344\n\t" \
  "v_mfma_f32_16x16x32_bf16 v[76:79], v[144:147], v[140:143], v[76:79]\n\t" \
  "ds_read_b128 v[164:167], %[pa] offset:57600\n\t" \
  "v_mfma_f32_16x16x32_bf16 v[80:83], v[148:151], v[128:131], v[80:83]\n\t" \
  "ds_read_b128 v[168:171], %[pa] offset:57856\n\t" \
  "v_mfma_f32_16x16x32_bf16 v[84:87], v[148:151], v[132:135], v[84:87]\n\t" \
  "ds_read_b128 v[172:175], %[pa] offset:58112\n\t" \
  "v_mfma_f32_16x16x32_bf16 v[88:91], v[148:151], v[136:139], v[88:91]\n\t" \
  "ds_read_b64_tr_b16 v[180:181], %[ta1] offset:57344\n\t" \
  "v_mfma_f32_16x16x32_bf16 v[92:95], v[148:151], v[140:143], v[92:95]\n\t" \
  "ds_read_b64_tr_b16 v[182:183], %[ta1] offset:61440\n\t" \
  "v_mfma_f32_16x16x32_bf16 v[96:99], v[152:155], v[128:131], v[96:99]\n\t" \
  "ds_read_b64_tr_b16 v[184:185], %[ta2] offset:57344\n\t" \
  "v_mfma_f32_16x16x32_bf16 v[100:103], v[152:155], v[132:135], v[100:103]\n\t" \
  "ds_read_b64_tr_b16 v[186:187], %[ta2] offset:61440\n\t" \
  "v_mfma_f32_16x16x32_bf16 v[104:107], v[152:155], v[136:139], v[104:107]\n\t" \
  "ds_read_b64_tr_b16 v[188:189], %[ta3] offset:57344\n\t" \
  "v_mfma_f32_16x16x32_bf16 v[108:111], v[152:155], v[140:143], v[108:111]\n\t" \
  "ds_read_b64_tr_b16 v[190:191], %[ta3] offset:61440\n\t" \
  "v_mfma_f32_16x16x32_bf16 v[112:115], v[156:159], v[128:131], v[112:115]\n\t" \
  "v_mfma_f32_16x16x32_bf16 v[116:119], v[156:159], v[132:135], v[116:119]\n\t" \
  "v_mfma_f32_16x16x32_bf16 v[120:123], v[156:159], v[136:139], v[120:123]\n\t" \
  "v_mfma_f32_16x16x32_bf16 v[124:127], v[156:159], v[140:143], v[124:127]\n\t" \
  "s_sub_u32 %[cnt], %[cnt], 1\n\t" \
  "s_cmp_eq_u32 %[cnt], 0\n\t" \
  "s_cbranch_scc1 2f\n\t" \
  "s_waitcnt vmcnt(0)\n\t" \
  "s_waitcnt lgkmcnt(0)\n\t" \
  "s_barrier\n\t" \
  "s_add_u32 %[tmp], %[cnt], %[wid]\n\t" \
  "s_cmp_lg_u32 %[tmp], %[nq]-1\n\t" \
  "s_cbranch_scc1 3f\n\t" \
  "s_cmp_lg_u32 %[wid], 0\n\t" \
  "s_cbranch_scc1 3f\n\t" \
  "s_mov_b64 exec, 0xff\n\t" \
  "global_store_dwordx2 %[zero], %[one], %[sig] sc1\n\t" \
  "s_mov_b64 exec, -1\n\t" \
  "3:\n\t" \
  "v_mfma_f32_16x16x32_bf16 v[64:67], v[176:179], v[160:163], v[64:67]\n\t" \
  "ds_read_b64_tr_b16 v[144:145], %[ta0] offset:0\n\t" \
  "v_mfma_f32_16x16x32_bf16 v[68:71], v[176:179], v[164:167], v[68:71]\n\t" \
  "ds_read_b64_tr_b16 v[146:147], %[ta0] offset:4096\n\t" \
  "v_mfma_f32_16x16x32_bf16 v[72:75], v[176:179], v[168:171], v[72:75]\n\t" \
  "ds_read_b128 v[128:131], %[pa] offset:0\n\t" \
  "v_mfma_f32_16x16x32_bf16 v[76:79], v[176:179], v[172:175], v[76:79]\n\t" \
  "ds_read_b128 v[132:135], %[pa] offset:256\n\t" \
  "v_mfma_f32_16x16x32_bf16 v[80:83], v[180:183], v[160:163], v[80:83]\n\t" \
  "ds_read_b128 v[136:139], %[pa] offset:512\n\t" \
  "v_mfma_f32_16x16x32_bf16 v[84:87], v[180:183], v[164:167], v[84:87]\n\t" \
  "ds_read_b128 v[140:143], %[pa] offset:768\n\t" \
  "v_mfma_f32_16x16x32_bf16 v[88:91], v[180:183], v[168:171], v[88:91]\n\t" \
  "ds_read_b64_tr_b16 v[148:149], %[ta1] offset:0\n\t" \
  "v_mfma_f32_16x16x32_bf16 v[92:95], v[180:183], v[172:175], v[92:95]\n\t" \
  "ds_read_b64_tr_b16 v[150:151], %[ta1] offset:4096\n\t" \
  "v_mfma_f32_16x16x32_bf16 v[96:99], v[184:187], v[160:163], v[96:99]\n\t" \
  "ds_read_b64_tr_b16 v[152:153], %[ta2] offset:0\n\t" \
  "v_mfma_f32_16x16x32_bf16 v[100:103], v[184:187], v[164:167], v[100:103]\n\t" \
  "ds_read_b64_tr_b16 v[154:155], %[ta2] offset:4096\n\t" \
  "v_mfma_f32_16x16x32_bf16 v[104:107], v[184:187], v[168:171], v[104:107]\n\t" \
  "ds_read_b64_tr_b16 v[156:157], %[ta3] offset:0\n\t" \
  "v_mfma_f32_16x16x32_bf16 v[108:111], v[184:187], v[172:175], v[108:111]\n\t" \
  "ds_read_b64_tr_b16 v[158:159], %[ta3] offset:4096\n\t" \
  "v_mfma_f32_16x16x32_bf16 v[112:115], v[188:191], v[160:163], v[112:115]\n\t" \
  "v_mfma_f32_16x16x32_bf16 v[116:119], v[188:191], v[164:167], v[116:119]\n\t" \
  "v_mfma_f32_16x16x32_bf16 v[120:123], v[188:191], v[168:171], v[120:123]\n\t" \
  "v_mfma_f32_16x16x32_bf16 v[124:127], v[188:191], v[172:175], v[124:127]\n\t" \
  "s_branch 1b\n\t" \
  "2:\n\t" \
  "s_waitcnt lgkmcnt(0)\n\t" \
  "v_mfma_f32_16x16x32_bf16 v[64:67], v[176:179], v[160:163], v[64:67]\n\t" \
  "v_mfma_f32_16x16x32_bf16 v[68:71], v[176:179], v[164:167], v[68:71]\n\t" \
  "v_mfma_f32_16x16x32_bf16 v[72:75], v[176:179], v[168:171], v[72:75]\n\t" \
  "v_mfma_f32_16x16x32_bf16 v[76:79], v[176:179], v[172:175], v[76:79]\n\t" \
  "v_mfma_f32_16x16x32_bf16 v[80:83], v[180:183], v[160:163], v[80:83]\n\t" \
  "v_mfma_f32_16x16x32_bf16 v[84:87], v[180:183], v[164:167], v[84:87]\n\t" \
  "v_mfma_f32_16x16x32_bf16 v[88:91], v[180:183], v[168:171], v[88:91]\n\t" \
  "v_mfma_f32_16x16x32_bf16 v[92:95], v[180:183], v[172:175], v[92:95]\n\t" \
  "v_mfma_f32_16x16x32_bf16 v[96:99], v[184:187], v[160:163], v[96:99]\n\t" \
  "v_mfma_f32_16x16x32_bf16 v[100:103], v[184:187], v[164:167], v[100:103]\n\t" \
  "v_mfma_f32_16x16x32_bf16 v[104:107], v[184:187], v[168:171], v[104:107]\n\t" \
  "v_mfma_f32_16x16x32_bf16 v[108:111], v[184:187], v[172:175], v[108:111]\n\t" \
  "v_mfma_f32_16x16x32_bf16 v[112:115], v[188:191], v[160:163], v[112:115]\n\t" \
  "v_mfma_f32_16x16x32_bf16 v[116:119], v[188:191], v[164:167], v[116:119]\n\t" \
  "v_mfma_f32_16x16x32_bf16 v[120:123], v[188:191], v[168:171], v[120:123]\n\t" \
  "v_mfma_f32_16x16x32_bf16 v[124:127], v[188:191], v[172:175], v[124:127]\n\t" \
  "s_nop 15\n\t"
#define KLOOP_PA \
  "ds_read_b64_tr_b16 v[144:145], %[ta0] offset:0\n\t" \
  "ds_read_b64_tr_b16 v[146:147], %[ta0] offset:4096\n\t" \
  "ds_read_b128 v[128:131], %[pa] offset:0\n\t" \
  "ds_read_b128 v[132:135], %[pa] offset:256\n\t" \
  "ds_read_b128 v[136:139], %[pa] offset:512\n\t" \
  "ds_read_b128 v[140:143], %[pa] offset:768\n\t" \
  "ds_read_b64_tr_b16 v[148:149], %[ta1] offset:0\n\t" \
  "ds_read_b64_tr_b16 v[150:151], %[ta1] offset:4096\n\t" \
  "ds_read_b64_tr_b16 v[152:153], %[ta2] offset:0\n\t" \
  "ds_read_b64_tr_b16 v[154:155], %[ta2] offset:4096\n\t" \
  "ds_read_b64_tr_b16 v[156:157], %[ta3] offset:0\n\t" \
  "ds_read_b64_tr_b16 v[158:159], %[ta3] offset:4096\n\t" \
  "s_mov_b32 %[cnt], %[nq]\n\t" \
  ".p2align 6\n\t" \
  "1:\n\t" \
  "s_waitcnt lgkmcnt(0)\n\t" \
  "v_mfma_f32_16x16x32_bf16 v[64:67], v[128:131], v[144:147], v[64:67]\n\t" \
  "ds_read_b64_tr_b16 v[176:177], %[ta0] offset:8192\n\t" \
  "v_mfma_f32_16x16x32_bf16 v[68:71], v[132:135], v[144:147], v[68:71]\n\t" \
  "ds_read_b64_tr_b16 v[178:179], %[ta0] offset:12288\n\t" \
  "v_mfma_f32_16x16x32_bf16 v[72:75], v[136:139], v[144:147], v[72:75]\n\t" \
  "ds_read_b128 v[160:163], %[pa] offset:8192\n\t" \
  "v_mfma_f32_16x16x32_bf16 v[76:79], v[140:143], v[144:147], v[76:79]\n\t" \
  "ds_read_b128 v[164:167], %[pa] offset:8448\n\t" \
  "v_mfma_f32_16x16x32_bf16 v[80:83], v[128:131], v[148:151], v[80:83]\n\t" \
  "ds_read_b128 v[168:171], %[pa] offset:8704\n\t" \
  "v_mfma_f32_16x16x32_bf16 v[84:87], v[132:135], v[148:151], v[84:87]\n\t" \
  "ds_read_b128 v[172:175], %[pa] offset:8960\n\t" \
  "v_mfma_f32_16x16x32_bf16 v[88:91], v[136:139], v[148:151], v[88:91]\n\t" \
  "ds_read_b64_tr_b16 v[180:181], %[ta1] offset:8192\n\t" \
  "v_mfma_f32_16x16x32_bf16 v[92:95], v[140:143], v[148:151], v[92:95]\n\t" \
  "ds_read_b64_tr_b16 v[182:183], %[ta1] offset:12288\n\t" \
  "v_mfma_f32_16x16x32_bf16 v[96:99], v[128:131], v[152:155], v[96:99]\n\t" \
  "ds_read_b64_tr_b16 v[184:185], %[ta2] offset:8192\n\t" \
  "v_mfma_f32_16x16x32_bf16 v[100:103], v[132:135], v[152:155], v[100:103]\n\t" \
  "ds_read_b64_tr_b16 v[186:187], %[ta2] offset:12288\n\t" \
  "v_mfma_f32_16x16x32_bf16 v[104:107], v[136:139], v[152:155], v[104:107]\n\t" \
  "ds_read_b64_tr_b16 v[188:189], %[ta3] offset:8192\n\t" \
  "v_mfma_f32_16x16x32_bf16 v[108:111], v[140:143], v[152:155], v[108:111]\n\t" \
  "ds_read_b64_tr_b16 v[190:191], %[ta3] offset:12288\n\t" \
  "v_mfma_f32_16x16x32_bf16 v[112:115], v[128:131], v[156:159], v[112:115]\n\t" \
  "v_mfma_f32_16x16x32_bf16 v[116:119], v[132:135], v[156:159], v[116:119]\n\t" \
  "v_mfma_f32_16x16x32_bf16 v[120:123], v[136:139], v[156:159], v[120:123]\n\t" \
  "v_mfma_f32_16x16x32_bf16 v[124:127], v[140:143], v[156:159], v[124:127]\n\t" \
  "s_waitcnt lgkmcnt(0)\n\t" \
  "s_barrier\n\t" \
  "v_mfma_f32_16x16x32_bf16 v[64:67], v[160:163], v[176:179], v[64:67]\n\t" \
  "ds_read_b64_tr_b16 v[144:145], %[ta0] offset:16384\n\t" \
  "v_mfma_f32_16x16x32_bf16 v[68:71], v[164:167], v[176:179], v[68:71]\n\t" \
  "ds_read_b64_tr_b16 v[146:147], %[ta0] offset:20480\n\t" \
  "v_mfma_f32_16x16x32_bf16 v[72:75], v[168:171], v[176:179], v[72:75]\n\t" \
  "ds_read_b128 v[128:131], %[pa] offset:16384\n\t" \
  "v_mfma_f32_16x16x32_bf16 v[76:79], v[172:175], v[176:179], v[76:79]\n\t" \
  "ds_read_b128 v[132:135], %[pa] offset:16640\n\t" \
  "v_mfma_f32_16x16x32_bf16 v[80:83], v[160:163], v[180:183], v[80:83]\n\t" \
  "ds_read_b128 v[136:139], %[pa] offset:16896\n\t" \
  "v_mfma_f32_16x16x32_bf16 v[84:87], v[164:167], v[180:183], v[84:87]\n\t" \
  "ds_read_b128 v[140:143], %[pa] offset:17152\n\t" \
  "v_mfma_f32_16x16x32_bf16 v[88:91], v[168:171], v[180:183], v[88:91]\n\t" \
  "ds_read_b64_tr_b16 v[148:149], %[ta1] offset:16384\n\t" \
  "v_mfma_f32_16x16x32_bf16 v[92:95], v[172:175], v[180:183], v[92:95]\n\t" \
  "ds_read_b64_tr_b16 v[150:151], %[ta1] offset:20480\n\t" \
  "v_mfma_f32_16x16x32_bf16 v[96:99], v[160:163], v[184:187], v[96:99]\n\t" \
  "ds_read_b64_tr_b16 v[152:153], %[ta2] offset:16384\n\t" \
  "v_mfma_f32_16x16x32_bf16 v[100:103], v[164:167], v[184:187], v[100:103]\n\t" \
  "ds_read_b64_tr_b16 v[154:155], %[ta2] offset:20480\n\t" \
  "v_mfma_f32_16x16x32_bf16 v[104:107], v[168:171], v[184:187], v[104:107]\n\t" \
  "ds_read_b64_tr_b16 v[156:157], %[ta3] offset:16384\n\t" \
  "v_mfma_f32_16x16x32_bf16 v[108:111], v[172:175], v[184:187], v[108:111]\n\t" \
  "ds_read_b64_tr_b16 v[158:159], %[ta3] offset:20480\n\t" \
  "v_mfma_f32_16x16x32_bf16 v[112:115], v[160:163], v[188:191], v[112:115]\n\t" \
  "v_mfma_f32_16x16x32_bf16 v[116:119], v[164:167], v[188:191], v[116:119]\n\t" \
  "v_mfma_f32_16x16x32_bf16 v[120:123], v[168:171], v[188:191], v[120:123]\n\t" \
  "v_mfma_f32_16x16x32_bf16 v[124:127], v[172:175], v[188:191], v[124:127]\n\t" \
  "s_waitcnt lgkmcnt(0)\n\t" \
  "v_mfma_f32_16x16x32_bf16 v[64:67], v[128:131], v[144:147], v[64:67]\n\t" \
  "ds_read_b64_tr_b16 v[176:177], %[ta0] offset:24576\n\t" \
  "v_mfma_f32_16x16x32_bf16 v[68:71], v[132:135], v[144:147], v[68:71]\n\t" \
  "ds_read_b64_tr_b16 v[178:179], %[ta0] offset:28672\n\t" \
  "v_mfma_f32_16x16x32_bf16 v[72:75], v[136:139], v[144:147], v[72:75]\n\t" \
  "ds_read_b128 v[160:163], %[pa] offset:24576\n\t" \
  "v_mfma_f32_16x16x32_bf16 v[76:79], v[140:143], v[144:147], v[76:79]\n\t" \
  "ds_read_b128 v[164:167], %[pa] offset:24832\n\t" \
  "v_mfma_f32_16x16x32_bf16 v[80:83], v[128:131], v[148:151], v[80:83]\n\t" \
  "ds_read_b128 v[168:171], %[pa] offset:25088\n\t" \
  "v_mfma_f32_16x16x32_bf16 v[84:87], v[132:135], v[148:151], v[84:87]\n\t" \
  "ds_read_b128 v[172:175], %[pa] offset:25344\n\t" \
  "v_mfma_f32_16x16x32_bf16 v[88:91], v[136:139], v[148:151], v[88:91]\n\t" \
  "ds_read_b64_tr_b16 v[180:181], %[ta1] offset:24576\n\t" \
  "v_mfma_f32_16x16x32_bf16 v[92:95], v[140:143], v[148:151], v[92:95]\n\t" \
  "ds_read_b64_tr_b16 v[182:183], %[ta1] offset:28672\n\t" \
  "v_mfma_f32_16x16x32_bf16 v[96:99], v[128:131], v[152:155], v[96:99]\n\t" \
  "ds_read_b64_tr_b16 v[184:185], %[ta2] offset:24576\n\t" \
  "v_mfma_f32_16x16x32_bf16 v[100:103], v[132:135], v[152:155], v[100:103]\n\t" \
  "ds_read_b64_tr_b16 v[186:187], %[ta2] offset:28672\n\t" \
  "v_mfma_f32_16x16x32_bf16 v[104:107], v[136:139], v[152:155], v[104:107]\n\t" \
  "ds_read_b64_tr_b16 v[188:189], %[ta3] offset:24576\n\t" \
  "v_mfma_f32_16x16x32_bf16 v[108:111], v[140:143], v[152:155], v[108:111]\n\t" \
  "ds_read_b64_tr_b16 v[190:191], %[ta3] offset:28672\n\t" \
  "v_mfma_f32_16x16x32_bf16 v[112:115], v[128:131], v[156:159], v[112:115]\n\t" \
  "v_mfma_f32_16x16x32_bf16 v[116:119], v[132:135], v[156:159], v[116:119]\n\t" \
  "v_mfma_f32_16x16x32_bf16 v[120:123], v[136:139], v[156:159], v[120:123]\n\t" \
  "v_mfma_f32_16x16x32_bf16 v[124:127], v[140:143], v[156:159], v[124:127]\n\t" \
  "s_waitcnt lgkmcnt(0)\n\t" \
  "s_barrier\n\t" \
  "v_mfma_f32_16x16x32_bf16 v[64:67], v[160:163], v[176:179], v[64:67]\n\t" \
  "ds_read_b64_tr_b16 v[144:145], %[ta0] offset:32768\n\t" \
  "v_mfma_f32_16x16x32_bf16 v[68:71], v[164:167], v[176:179], v[68:71]\n\t" \
  "ds_read_b64_tr_b16 v[146:147], %[ta0] offset:36864\n\t" \
  "v_mfma_f32_16x16x32_bf16 v[72:75], v[168:171], v[176:179], v[72:75]\n\t" \
  "ds_read_b128 v[128:131], %[pa] offset:32768\n\t" \
  "v_mfma_f32_16x16x32_bf16 v[76:79], v[172:175], v[176:179], v[76:79]\n\t" \
  "ds_read_b128 v[132:135], %[pa] offset:33024\n\t" \
  "v_mfma_f32_16x16x32_bf16 v[80:83], v[160:163], v[180:183], v[80:83]\n\t" \
  "ds_read_b128 v[136:139], %[pa] offset:33280\n\t" \
  "v_mfma_f32_16x16x32_bf16 v[84:87], v[164:167], v[180:183], v[84:87]\n\t" \
  "ds_read_b128 v[140:143], %[pa] offset:33536\n\t" \
  "v_mfma_f32_16x16x32_bf16 v[88:91], v[168:171], v[180:183], v[88:91]\n\t" \
  "ds_read_b64_tr_b16 v[148:149], %[ta1] offset:32768\n\t" \
  "v_mfma_f32_16x16x32_bf16 v[92:95], v[172:175], v[180:183], v[92:95]\n\t" \
  "ds_read_b64_tr_b16 v[150:151], %[ta1] offset:36864\n\t" \
  "v_mfma_f32_16x16x32_bf16 v[96:99], v[160:163], v[184:187], v[96:99]\n\t" \
  "ds_read_b64_tr_b16 v[152:153], %[ta2] offset:32768\n\t" \
  "v_mfma_f32_16x16x32_bf16 v[100:103], v[164:167], v[184:187], v[100:103]\n\t" \
  "ds_read_b64_tr_b16 v[154:155], %[ta2] offset:36864\n\t" \
  "v_mfma_f32_16x16x32_bf16 v[104:107], v[168:171], v[184:187], v[104:107]\n\t" \
  "ds_read_b64_tr_b16 v[156:157], %[ta3] offset:32768\n\t" \
  "v_mfma_f32_16x16x32_bf16 v[108:111], v[172:175], v[184:187], v[108:111]\n\t" \
  "ds_read_b64_tr_b16 v[158:159], %[ta3] offset:36864\n\t" \
  "v_mfma_f32_16x16x32_bf16 v[112:115], v[160:163], v[188:191], v[112:115]\n\t" \
  "v_mfma_f32_16x16x32_bf16 v[116:119], v[164:167], v[188:191], v[116:119]\n\t" \
  "v_mfma_f32_16x16x32_bf16 v[120:123], v[168:171], v[188:191], v[120:123]\n\t" \
  "v_mfma_f32_16x16x32_bf16 v[124:127], v[172:175], v[188:191], v[124:127]\n\t" \
  "s_waitcnt lgkmcnt(0)\n\t" \
  "v_mfma_f32_16x16x32_bf16 v[64:67], v[128:131], v[144:147], v[64:67]\n\t" \
  "ds_read_b64_tr_b16 v[176:177], %[ta0] offset:40960\n\t" \
  "v_mfma_f32_16x16x32_bf16 v[68:71], v[132:135], v[144:147], v[68:71]\n\t" \
  "ds_read_b64_tr_b16 v[178:179], %[ta0] offset:45056\n\t" \
  "v_mfma_f32_16x16x32_bf16 v[72:75], v[136:139], v[144:147], v[72:75]\n\t" \
  "ds_read_b128 v[160:163], %[pa] offset:40960\n\t" \
  "v_mfma_f32_16x16x32_bf16 v[76:79], v[140:143], v[144:147], v[76:79]\n\t" \
  "ds_read_b128 v[164:167], %[pa] offset:41216\n\t" \
  "v_mfma_f32_16x16x32_bf16 v[80:83], v[128:131], v[148:151], v[80:83]\n\t" \
  "ds_read_b128 v[168:171], %[pa] offset:41472\n\t" \
  "v_mfma_f32_16x16x32_bf16 v[84:87], v[132:135], v[148:151], v[84:87]\n\t" \
  "ds_read_b128 v[172:175], %[pa] offset:41728\n\t" \
  "v_mfma_f32_16x16x32_bf16 v[88:91], v[136:139], v[148:151], v[88:91]\n\t" \
  "ds_read_b64_tr_b16 v[180:181], %[ta1] offset:40960\n\t" \
  "v_mfma_f32_16x16x32_bf16 v[92:95], v[140:143], v[148:151], v[92:95]\n\t" \
  "ds_read_b64_tr_b16 v[182:183], %[ta1] offset:45056\n\t" \
  "v_mfma_f32_16x16x32_bf16 v[96:99], v[128:131], v[152:155], v[96:99]\n\t" \
  "ds_read_b64_tr_b16 v[184:185], %[ta2] offset:40960\n\t" \
  "v_mfma_f32_16x16x32_bf16 v[100:103], v[132:135], v[152:155], v[100:103]\n\t" \
  "ds_read_b64_tr_b16 v[186:187], %[ta2] offset:45056\n\t" \
  "v_mfma_f32_16x16x32_bf16 v[104:107], v[136:139], v[152:155], v[104:107]\n\t" \
  "ds_read_b64_tr_b16 v[188:189], %[ta3] offset:40960\n\t" \
  "v_mfma_f32_16x16x32_bf16 v[108:111], v[140:143], v[152:155], v[108:111]\n\t" \
  "ds_read_b64_tr_b16 v[190:191], %[ta3] offset:45056\n\t" \
  "v_mfma_f32_16x16x32_bf16 v[112:115], v[128:131], v[156:159], v[112:115]\n\t" \
  "v_mfma_f32_16x16x32_bf16 v[116:119], v[132:135], v[156:159], v[116:119]\n\t" \
  "v_mfma_f32_16x16x32_bf16 v[120:123], v[136:139], v[156:159], v[120:123]\n\t" \
  "v_mfma_f32_16x16x32_bf16 v[124:127], v[140:143], v[156:159], v[124:127]\n\t" \
  "s_waitcnt lgkmcnt(0)\n\t" \
  "s_barrier\n\t" \
  "v_mfma_f32_16x16x32_bf16 v[64:67], v[160:163], v[176:179], v[64:67]\n\t" \
  "ds_read_b64_tr_b16 v[144:145], %[ta0] offset:49152\n\t" \
  "v_mfma_f32_16x16x32_bf16 v[68:71], v[164:167], v[176:179], v[68:71]\n\t" \
  "ds_read_b64_tr_b16 v[146:147], %[ta0] offset:53248\n\t" \
  "v_mfma_f32_16x16x32_bf16 v[72:75], v[168:171], v[176:179], v[72:75]\n\t" \
  "ds_read_b128 v[128:131], %[pa] offset:49152\n\t" \
  "v_mfma_f32_16x16x32_bf16 v[76:79], v[172:175], v[176:179], v[76:79]\n\t" \
  "ds_read_b128 v[132:135], %[pa] offset:49408\n\t" \
  "v_mfma_f32_16x16x32_bf16 v[80:83], v[160:163], v[180:183], v[80:83]\n\t" \
  "ds_read_b128 v[136:139], %[pa] offset:49664\n\t" \
  "v_mfma_f32_16x16x32_bf16 v[84:87], v[164:167], v[180:183], v[84:87]\n\t" \
  "ds_read_b128 v[140:143], %[pa] offset:49920\n\t" \
  "v_mfma_f32_16x16x32_bf16 v[88:91], v[168:171], v[180:183], v[88:91]\n\t" \
  "ds_read_b64_tr_b16 v[148:149], %[ta1] offset:49152\n\t" \
  "v_mfma_f32_16x16x32_bf16 v[92:95], v[172:175], v[180:183], v[92:95]\n\t" \
  "ds_read_b64_tr_b16 v[150:151], %[ta1] offset:53248\n\t" \
  "v_mfma_f32_16x16x32_bf16 v[96:99], v[160:163], v[184:187], v[96:99]\n\t" \
  "ds_read_b64_tr_b16 v[152:153], %[ta2] offset:49152\n\t" \
  "v_mfma_f32_16x16x32_bf16 v[100:103], v[164:167], v[184:187], v[100:103]\n\t" \
  "ds_read_b64_tr_b16 v[154:155], %[ta2] offset:53248\n\t" \
  "v_mfma_f32_16x16x32_bf16 v[104:107], v[168:171], v[184:187], v[104:107]\n\t" \
  "ds_read_b64_tr_b16 v[156:157], %[ta3] offset:49152\n\t" \
  "v_mfma_f32_16x16x32_bf16 v[108:111], v[172:175], v[184:187], v[108:111]\n\t" \
  "ds_read_b64_tr_b16 v[158:159], %[ta3] offset:53248\n\t" \
  "v_mfma_f32_16x16x32_bf16 v[112:115], v[160:163], v[188:191], v[112:115]\n\t" \
  "v_mfma_f32_16x16x32_bf16 v[116:119], v[164:167], v[188:191], v[116:119]\n\t" \
  "v_mfma_f32_16x16x32_bf16 v[120:123], v[168:171], v[188:191], v[120:123]\n\t" \
  "v_mfma_f32_16x16x32_bf16 v[124:127], v[172:175], v[188:191], v[124:127]\n\t" \
  "s_waitcnt lgkmcnt(0)\n\t" \
  "v_mfma_f32_16x16x32_bf16 v[64:67], v[128:131], v[144:147], v[64:67]\n\t" \
  "ds_read_b64_tr_b16 v[176:177], %[ta0] offset:57344\n\t" \
  "v_mfma_f32_16x16x32_bf16 v[68:71], v[132:135], v[144:147], v[68:71]\n\t" \
  "ds_read_b64_tr_b16 v[178:179], %[ta0] offset:61440\n\t" \
  "v_mfma_f32_16x16x32_bf16 v[72:75], v[136:139], v[144:147], v[72:75]\n\t" \
  "ds_read_b128 v[160:163], %[pa] offset:57344\n\t" \
  "v_mfma_f32_16x16x32_bf16 v[76:79], v[140:143], v[144:147], v[76:79]\n\t" \
  "ds_read_b128 v[164:167], %[pa] offset:57600\n\t" \
  "v_mfma_f32_16x16x32_bf16 v[80:83], v[128:131], v[148:151], v[80:83]\n\t" \
  "ds_read_b128 v[168:171], %[pa] offset:57856\n\t" \
  "v_mfma_f32_16x16x32_bf16 v[84:87], v[132:135], v[148:151], v[84:87]\n\t" \
  "ds_read_b128 v[172:175], %[pa] offset:58112\n\t" \
  "v_mfma_f32_16x16x32_bf16 v[88:91], v[136:139], v[148:151], v[88:91]\n\t" \
  "ds_read_b64_tr_b16 v[180:181], %[ta1] offset:57344\n\t" \
  "v_mfma_f32_16x16x32_bf16 v[92:95], v[140:143], v[148:151], v[92:95]\n\t" \
  "ds_read_b64_tr_b16 v[182:183], %[ta1] offset:61440\n\t" \
  "v_mfma_f32_16x16x32_bf16 v[96:99], v[128:131], v[152:155], v[96:99]\n\t" \
  "ds_read_b64_tr_b16 v[184:185], %[ta2] offset:57344\n\t" \
  "v_mfma_f32_16x16x32_bf16 v[100:103], v[132:135], v[152:155], v[100:103]\n\t" \
  "ds_read_b64_tr_b16 v[186:187], %[ta2] offset:61440\n\t" \
  "v_mfma_f32_16x16x32_bf16 v[104:107], v[136:139], v[152:155], v[104:107]\n\t" \
  "ds_read_b64_tr_b16 v[188:189], %[ta3] offset:57344\n\t" \
  "v_mfma_f32_16x16x32_bf16 v[108:111], v[140:143], v[152:155], v[108:111]\n\t" \
  "ds_read_b64_tr_b16 v[190:191], %[ta3] offset:61440\n\t" \
  "v_mfma_f32_16x16x32_bf16 v[112:115], v[128:131], v[156:159], v[112:115]\n\t" \
  "v_mfma_f32_16x16x32_bf16 v[116:119], v[132:135], v[156:159], v[116:119]\n\t" \
  "v_mfma_f32_16x16x32_bf16 v[120:123], v[136:139], v[156:159], v[120:123]\n\t" \
  "v_mfma_f32_16x16x32_bf16 v[124:127], v[140:143], v[156:159], v[124:127]\n\t" \
  "s_sub_u32 %[cnt], %[cnt], 1\n\t" \
  "s_cmp_eq_u32 %[cnt], 0\n\t" \
  "s_cbranch_scc1 2f\n\t" \
  "s_waitcnt lgkmcnt(0)\n\t" \
  "s_barrier\n\t" \
  "v_mfma_f32_16x16x32_bf16 v[64:67], v[160:163], v[176:179], v[64:67]\n\t" \
  "ds_read_b64_tr_b16 v[144:145], %[ta0] offset:0\n\t" \
  "v_mfma_f32_16x16x32_bf16 v[68:71], v[164:167], v[176:179], v[68:71]\n\t" \
  "ds_read_b64_tr_b16 v[146:147], %[ta0] offset:4096\n\t" \
  "v_mfma_f32_16x16x32_bf16 v[72:75], v[168:171], v[176:179], v[72:75]\n\t" \
  "ds_read_b128 v[128:131], %[pa] offset:0\n\t" \
  "v_mfma_f32_16x16x32_bf16 v[76:79], v[172:175], v[176:179], v[76:79]\n\t" \
  "ds_read_b128 v[132:135], %[pa] offset:256\n\t" \
  "v_mfma_f32_16x16x32_bf16 v[80:83], v[160:163], v[180:183], v[80:83]\n\t" \
  "ds_read_b128 v[136:139], %[pa] offset:512\n\t" \
  "v_mfma_f32_16x16x32_bf16 v[84:87], v[164:167], v[180:183], v[84:87]\n\t" \
  "ds_read_b128 v[140:143], %[pa] offset:768\n\t" \
  "v_mfma_f32_16x16x32_bf16 v[88:91], v[168:171], v[180:183], v[88:91]\n\t" \
  "ds_read_b64_tr_b16 v[148:149], %[ta1] offset:0\n\t" \
  "v_mfma_f32_16x16x32_bf16 v[92:95], v[172:175], v[180:183], v[92:95]\n\t" \
  "ds_read_b64_tr_b16 v[150:151], %[ta1] offset:4096\n\t" \
  "v_mfma_f32_16x16x32_bf16 v[96:99], v[160:163], v[184:187], v[96:99]\n\t" \
  "ds_read_b64_tr_b16 v[152:153], %[ta2] offset:0\n\t" \
  "v_mfma_f32_16x16x32_bf16 v[100:103], v[164:167], v[184:187], v[100:103]\n\t" \
  "ds_read_b64_tr_b16 v[154:155], %[ta2] offset:4096\n\t" \
  "v_mfma_f32_16x16x32_bf16 v[104:107], v[168:171], v[184:187], v[104:107]\n\t" \
  "ds_read_b64_tr_b16 v[156:157], %[ta3] offset:0\n\t" \
  "v_mfma_f32_16x16x32_bf16 v[108:111], v[172:175], v[184:187], v[108:111]\n\t" \
  "ds_read_b64_tr_b16 v[158:159], %[ta3] offset:4096\n\t" \
  "v_mfma_f32_16x16x32_bf16 v[112:115], v[160:163], v[188:191], v[112:115]\n\t" \
  "v_mfma_f32_16x16x32_bf16 v[116:119], v[164:167], v[188:191], v[116:119]\n\t" \
  "v_mfma_f32_16x16x32_bf16 v[120:123], v[168:171], v[188:191], v[120:123]\n\t" \
  "v_mfma_f32_16x16x32_bf16 v[124:127], v[172:175], v[188:191], v[124:127]\n\t" \
  "s_branch 1b\n\t" \
  "2:\n\t" \
  "s_waitcnt lgkmcnt(0)\n\t" \
  "v_mfma_f32_16x16x32_bf16 v[64:67], v[160:163], v[176:179], v[64:67]\n\t" \
  "v_mfma_f32_16x16x32_bf16 v[68:71], v[164:167], v[176:179], v[68:71]\n\t" \
  "v_mfma_f32_16x16x32_bf16 v[72:75], v[168:171], v[176:179], v[72:75]\n\t" \
  "v_mfma_f32_16x16x32_bf16 v[76:79], v[172:175], v[176:179], v[76:79]\n\t" \
  "v_mfma_f32_16x16x32_bf16 v[80:83], v[160:163], v[180:183], v[80:83]\n\t" \
  "v_mfma_f32_16x16x32_bf16 v[84:87], v[164:167], v[180:183], v[84:87]\n\t" \
  "v_mfma_f32_16x16x32_bf16 v[88:91], v[168:171], v[180:183], v[88:91]\n\t" \
  "v_mfma_f32_16x16x32_bf16 v[92:95], v[172:175], v[180:183], v[92:95]\n\t" \
  "v_mfma_f32_16x16x32_bf16 v[96:99], v[160:163], v[184:187], v[96:99]\n\t" \
  "v_mfma_f32_16x16x32_bf16 v[100:103], v[164:167], v[184:187], v[100:103]\n\t" \
  "v_mfma_f32_16x16x32_bf16 v[104:107], v[168:171], v[184:187], v[104:107]\n\t" \
  "v_mfma_f32_16x16x32_bf16 v[108:111], v[172:175], v[184:187], v[108:111]\n\t" \
  "v_mfma_f32_16x16x32_bf16 v[112:115], v[160:163], v[188:191], v[112:115]\n\t" \
  "v_mfma_f32_16x16x32_bf16 v[116:119], v[164:167], v[188:191], v[116:119]\n\t" \
  "v_mfma_f32_16x16x32_bf16 v[120:123], v[168:171], v[188:191], v[120:123]\n\t" \
  "v_mfma_f32_16x16x32_bf16 v[124:127], v[172:175], v[188:191], v[124:127]\n\t" \
  "s_nop 15\n\t"
#define KLOOP_CLOB "v128","v129","v130","v131","v132","v133","v134","v135","v136","v137","v138","v139","v140","v141","v142","v143","v144","v145","v146","v147","v148","v149","v150","v151","v152","v153","v154","v155","v156","v157","v158","v159","v160","v161","v162","v163","v164","v165","v166","v167","v168","v169","v170","v171","v172","v173","v174","v175","v176","v177","v178","v179","v180","v181","v182","v183","v184","v185","v186","v187","v188","v189","v190","v191"

typedef float f32x2 __attribute__((ext_vector_type(2)));
typedef float f32x4 __attribute__((ext_vector_type(4)));
typedef float f32x16 __attribute__((ext_vector_type(16)));
typedef unsigned u32x2 __attribute__((ext_vector_type(2)));
typedef unsigned u32x4 __attribute__((ext_vector_type(4)));
typedef short s16x4 __attribute__((ext_vector_type(4)));
typedef __bf16 bf16x2 __attribute__((ext_vector_type(2)));
typedef __bf16 bf16x8 __attribute__((ext_vector_type(8)));
typedef unsigned long long u64;
typedef __amdgpu_buffer_rsrc_t rsrc_t;
#define DI __device__ __forceinline__
#define LAS __attribute__((address_space(3)))

constexpr unsigned WS_EP = 0;
constexpr unsigned WS_FH = 4096;
constexpr unsigned WS_FA = 12288;
constexpr unsigned WS_FZ = 28672;
constexpr unsigned WS_FR = 45056;
constexpr unsigned WS_FG = 47104;
constexpr unsigned WS_FS = 57344;
constexpr unsigned WS_H  = 1 << 16;
constexpr unsigned WS_X1 = WS_H + (4u << 20);
constexpr unsigned WS_WT = WS_X1 + 257 * 16384;
constexpr unsigned WS_G  = WS_WT + 257 * 16384;
#define ALIAS 0
constexpr unsigned WS_END = WS_G + 448 * 16384 + (ALIAS ? 0 : (8u << 20));
constexpr unsigned WS_Z1 = WS_X1;
constexpr unsigned WS_R  = ALIAS ? WS_WT : WS_G + 448 * 16384;
constexpr unsigned WS_XC = ALIAS ? WS_X1 : WS_R + (4u << 20);
constexpr unsigned L_T = 65536, L_AUX = 131072, L_SIZE = 163840;
constexpr int SPIN_CAP = 1 << 18;
constexpr unsigned EMPTY = 0xffffffffu;
#define OUTPOL 16
#define CODE_BYTES 55000
#define RETOUCH 8
#define PF1 2
#define PF2 6
#define PF3 14
#define ST2 14
#define ST3 32
#define LEAD 2
#define WLATE 0
#define FIRST 1
#define LDELAY 0

#define STAMP(K, P, ROLE) do {} while (0)
#define LSTAMP(I) do {} while (0)
#define LDUMP(K) do {} while (0)

DI unsigned cvtpk(float lo, float hi) { f32x2 v = {lo, hi}; return __builtin_bit_cast(unsigned, __builtin_convertvector(v, bf16x2)); }
DI float bflo(unsigned u) { return __uint_as_float(u << 16); }
DI float bfhi(unsigned u) { return __uint_as_float(u & 0xffff0000u); }
DI f32x4 mm32(u32x4 a, u32x4 b, f32x4 c) { return __builtin_amdgcn_mfma_f32_16x16x32_bf16(__builtin_bit_cast(bf16x8, a), __builtin_bit_cast(bf16x8, b), c, 0, 0, 0); }
DI u32x2 trread(unsigned addr) { return __builtin_bit_cast(u32x2, __builtin_amdgcn_ds_read_tr16_b64_v4i16((LAS s16x4*)(size_t)addr)); }
DI u32x4 cat(u32x2 a, u32x2 b) { return (u32x4){a.x, a.y, b.x, b.y}; }
DI u32x4 ldsr(unsigned a) { return *(LAS u32x4*)(size_t)a; }
DI void ldsw(unsigned a, u32x4 v) { *(LAS u32x4*)(size_t)a = v; }
template <int DEN> DI void cs(int num, float& c, float& s) { const float t = (float)(num & (DEN - 1)) * (1.0f / DEN); c = __builtin_amdgcn_cosf(t); s = __builtin_amdgcn_sinf(t); }
DI void glds(unsigned voff, const char* sbase, unsigned lds_off) {
  asm volatile("s_mov_b32 m0, %2\n\ts_nop 0\n\tglobal_load_lds_dwordx4 %0, %1" :: "v"(voff), "s"(sbase), "s"(lds_off) : "memory");
}
DI void glds1(unsigned voff, const char* sbase, unsigned lds_off) {
  asm volatile("s_mov_b32 m0, %2\n\ts_nop 0\n\tglobal_load_lds_dwordx4 %0, %1 sc1" :: "v"(voff), "s"(sbase), "s"(lds_off) : "memory");
}
DI void ld1_async(u32x4& v, unsigned voff, const char* sbase) { asm volatile("global_load_dwordx4 %0, %1, %2 sc1" : "=&v"(v) : "v"(voff), "s"(sbase) : "memory"); }
DI void touch(unsigned voff, const char* sbase) { asm volatile("global_load_dword v255, %0, %1" :: "v"(voff), "s"(sbase) : "memory", "v255"); }
DI void st1(rsrc_t rs, u32x4 v, unsigned voff, unsigned soff) { __builtin_amdgcn_raw_buffer_store_b128(v, rs, voff + soff, 0, 16); }
DI void raise(char* ws, unsigned off, u64 tag) { __hip_atomic_store((u64*)(ws + off), tag, __ATOMIC_RELAXED, __HIP_MEMORY_SCOPE_AGENT); }
DI int fresh(int lane) { asm volatile("" : "+v"(lane)); return lane; }
DI u64 next_tag(u32x2 e) {
  u64 t = ((u64)(unsigned)__builtin_amdgcn_readfirstlane(e.y) << 32 | (unsigned)__builtin_amdgcn_readfirstlane(e.x)) + 1;
  asm volatile("" : "+s"(t) :: "memory");
  return t;
}
DI bool both(u32x4 f, u64 tag) { return f.x == (unsigned)tag && f.y == (unsigned)(tag >> 32) && f.z == (unsigned)tag && f.w == (unsigned)(tag >> 32); }
template <int NP, int NAP = 3> DI void wait_flags(rsrc_t rs, unsigned off, int lane, u64 tag) {
  const unsigned vo = (lane < NP ? lane : 0) * 16;
  for (int spins = 0; spins < SPIN_CAP; ++spins) {
    bool ok = both(__builtin_amdgcn_raw_buffer_load_b128(rs, vo, off, 16), tag);
    #pragma unroll
    for (int i = 1; i < NP / 64; ++i) ok &= both(__builtin_amdgcn_raw_buffer_load_b128(rs, vo + i * 1024, off, 16), tag);
    if (__all(ok)) break;
    asm volatile("s_sleep %0" :: "i"(NAP) : "memory");
  }
  asm volatile("" ::: "memory");
}
DI float sum_a(float v) {
  v += __builtin_bit_cast(float, __builtin_amdgcn_update_dpp(0, __builtin_bit_cast(int, v), 0x124, 0xf, 0xf, false));
  return v + __builtin_bit_cast(float, __builtin_amdgcn_update_dpp(0, __builtin_bit_cast(int, v), 0x128, 0xf, 0xf, false));
}
DI float row_sum(float v) {
  v = sum_a(v);
  auto a = __builtin_amdgcn_permlane16_swap(__float_as_uint(v), __float_as_uint(v), false, false);
  v = __uint_as_float(a[0]) + __uint_as_float(a[1]);
  auto b = __builtin_amdgcn_permlane32_swap(__float_as_uint(v), __float_as_uint(v), false, false);
  return __uint_as_float(b[0]) + __uint_as_float(b[1]);
}
constexpr f32x4 Z4 = {0.f, 0.f, 0.f, 0.f};
#define BAR() asm volatile("s_barrier" ::: "memory")
#define WAIT_BAR(W) asm volatile("s_waitcnt " W "\n\ts_barrier" ::: "memory")
#define WAIT(W) asm volatile("s_waitcnt " W ::: "memory")

constexpr int HROW = 2112;
typedef unsigned u32x2u __attribute__((ext_vector_type(2), aligned(2)));
DI unsigned e1_off(int k, int w) { const int k1 = k >> 5; return (k ^ ((k >> 2) & 3) ^ ((k1 >> 2) & 1)) * 64 + ((w ^ k1) & 3) * 16; }
DI unsigned wt_off(int j1, int piece) { const int l = (j1 & 15) + 16 * (j1 >> 5); return (((j1 >> 4) & 1) * 64 + l) * 128 + (piece ^ ((l >> 1) & 7)) * 16; }

struct Retile { u32x2 vr[4], vi[4]; unsigned ha, hb; };
DI void retile_load(Retile& T, int gw, int lane, const unsigned short* wr, const unsigned short* wi) {
  T.ha = T.hb = 0;
  if ((gw & 7) == 0) {
    const int jj = 2 * gw + 4 * ((lane >> 3) & 3) + (lane & 3), col = (lane & 32) ? 512 : 0;
    const unsigned short* src = (lane & 4) ? wi : wr;
    asm volatile("global_load_ushort %0, %2, off\n\tglobal_load_ushort %1, %3, off" : "+v"(T.ha), "+v"(T.hb) : "v"(src + jj * 513 + col), "v"(src + ((2048 - jj) & 2047) * 513 + col) : "memory");
  }
  #pragma unroll
  for (int i = 0; i < 4; ++i) {
    const int j = 2 * gw + (i >> 1), e = (i & 1) ? ((2048 - j) & 2047) * 513 + 508 - 4 * lane : j * 513 + 1 + 4 * lane;
    asm volatile("global_load_dwordx2 %0, %2, %3\n\tglobal_load_dwordx2 %1, %2, %4" : "=&v"(T.vr[i]), "=&v"(T.vi[i]) : "v"(e * 2), "s"(wr), "s"(wi) : "memory");
  }
}
DI void retile_store(Retile& T, int gw, int lane, unsigned buf, rsrc_t rs) {
  asm volatile("" : "+v"(T.vr[0]), "+v"(T.vr[1]), "+v"(T.vr[2]), "+v"(T.vr[3]), "+v"(T.vi[0]), "+v"(T.vi[1]), "+v"(T.vi[2]), "+v"(T.vi[3]), "+v"(T.ha), "+v"(T.hb));
  #pragma unroll
  for (int q = 0; q < 2; ++q) {
    const int j = 2 * gw + q;
    const u32x4 v = cat(T.vr[2 * q + 1], T.vi[2 * q + 1]);
    ldsw(buf + (lane >> 2) * 128 + (lane & 3) * 16, cat(T.vr[2 * q], T.vi[2 * q]));
    ldsw(buf + (lane >> 2) * 128 + (lane & 3) * 16 + 64, (u32x4){__builtin_amdgcn_alignbit(v.y, v.y, 16), __builtin_amdgcn_alignbit(v.x, v.x, 16), __builtin_amdgcn_alignbit(v.w, v.w, 16), __builtin_amdgcn_alignbit(v.z, v.z, 16)});
    #pragma unroll
    for (int i = 0; i < 2; ++i) st1(rs, ldsr(buf + i * 1024 + lane * 16), (lane >> 3) * 16384 + wt_off(j >> 4, lane & 7), WS_WT + ((j & 15) * 16 + i * 8) * 16384);
  }
  const float v = (lane & 4) ? ((lane & 32) ? -0.5f : 0.5f) * (bflo(T.ha) - bflo(T.hb)) : 0.5f * (bflo(T.ha) + bflo(T.hb));
  if ((gw & 7) == 0) __builtin_amdgcn_raw_buffer_store_b16((unsigned short)cvtpk(v, v), rs, WS_WT + 256 * 16384 + wt_off(gw >> 3, lane >> 3) + (lane & 7) * 2, 0, 16);
}
DI bool sync4(unsigned word, unsigned target, int lane) {
  WAIT("lgkmcnt(0)");
  unsigned old = 0;
  if (lane == 0) old = __hip_atomic_fetch_add((LAS unsigned*)(size_t)word, 1u, __ATOMIC_RELAXED, __HIP_MEMORY_SCOPE_WORKGROUP);
  return (unsigned)__builtin_amdgcn_readfirstlane(old) + 1 == target;
}
DI void spin_lds(unsigned word, unsigned target) {
  for (int spins = 0; spins < SPIN_CAP * 16 && *(volatile LAS unsigned*)(size_t)word < target; ++spins) asm volatile("s_sleep 0" ::: "memory");
  asm volatile("" ::: "memory");
}

constexpr unsigned L_CNT = 49152 + 64, L_CNT2 = L_CNT + 64, L_HOLD = L_CNT + 128;
DI void rows_job(u32x4 (&xo)[9], bool hjob, int s0, int w, int lane, unsigned lb, const char* __restrict__ x, rsrc_t rs, char* ws, u32x2 epoch) {
  const int li = lane & 15, g = lane >> 4, a = li >> 2, rl = li & 3;
  #pragma unroll
  for (int i = 0; i < 8; ++i) glds(lane * 16, x + (size_t)(s0 + 128 * (4 * w + (i >> 1))) * 2048 + (i & 1) * 1024, lb + (4 * w + (i >> 1)) * HROW + (i & 1) * 1024);
  BAR();
  u32x4 F1[2][2][2], F2[2], Fc[2]; float twc[2][4], tws[2][4];
  if (!hjob) {
    #pragma unroll
    for (int t = 0; t < 2; ++t)
      #pragma unroll
      for (int jj = 0; jj < 4; ++jj) {
        float c, s; cs<32>(li * (4 * (4 * t + jj) + g), c, s);
        F1[0][0][t][jj] = cvtpk(c, s); F1[0][1][t][jj] = cvtpk(-s, c);
        const unsigned sg = (g & 1) ? 0x80008000u : 0u;
        F1[1][0][t][jj] = F1[0][0][t][jj] ^ sg; F1[1][1][t][jj] = F1[0][1][t][jj] ^ sg;
      }
    #pragma unroll
    for (int nl = 0; nl < 4; ++nl) {
      float c, s; cs<16>(li * (4 * g + nl), c, s);
      F2[0][nl] = cvtpk(c, s); F2[1][nl] = cvtpk(-s, c) ^ (li >= 8 ? 0x80008000u : 0u);
      cs<2048>((s0 + 128 * (4 * g + nl)) * li, c, s);
      Fc[0][nl] = cvtpk(c, s); Fc[1][nl] = cvtpk(-s, c);
      cs<512>(li * (4 * g + nl), twc[0][nl], tws[0][nl]); cs<512>((li + 16) * (4 * g + nl), twc[1][nl], tws[1][nl]);
    }
    #pragma unroll
    for (int h = 0; h < 2; ++h) {
      asm volatile("" : "+v"(F1[h][0][0]), "+v"(F1[h][0][1]), "+v"(F1[h][1][0]), "+v"(F1[h][1][1]), "+v"(F2[h]), "+v"(Fc[h]));
      asm volatile("" : "+v"(twc[h][0]), "+v"(twc[h][1]), "+v"(twc[h][2]), "+v"(twc[h][3]), "+v"(tws[h][0]), "+v"(tws[h][1]), "+v"(tws[h][2]), "+v"(tws[h][3]));
    }
  }
  WAIT("vmcnt(0)");
  STAMP(0, 1, hjob);
  const unsigned xa = lb + (4 * w + rl) * HROW + g * 64 + a * 16;
  u32x4 ch[8];
  #pragma unroll
  for (int i = 0; i < 8; ++i) ch[i] = ldsr(xa + i * 256);
  f32x4 sx = Z4, sxx = Z4;
  #pragma unroll
  for (int i = 0; i < 8; ++i) { sx = mm32((u32x4){0x3f803f80u, 0x3f803f80u, 0x3f803f80u, 0x3f803f80u}, ch[i], sx); sxx = mm32(ch[i], ch[i], sxx); }
  f32x2 d[8][4];
  #pragma unroll
  for (int i = 0; i < 8; ++i)
    #pragma unroll
    for (int e = 0; e < 4; ++e) d[i][e] = (f32x2){bflo(ch[i][e]), bfhi(ch[i][e])};
  const float dg = (rl & 2) ? ((rl & 1) ? sxx[3] : sxx[2]) : ((rl & 1) ? sxx[1] : sxx[0]);
  const float mean = sum_a(sx[0]) * (1.0f / 1024), var = fmaxf(row_sum(g == a ? dg : 0.f) * (1.0f / 1024) - mean * mean, 0.f);
  float rs1 = __builtin_amdgcn_rsqf(var + 1e-5f);
  if (hjob) rs1 *= __builtin_amdgcn_rsqf(var * rs1 * rs1 + 1e-5f);
  const float off = -mean * rs1;
  #pragma unroll
  for (int i = 0; i < 8; ++i)
    #pragma unroll
    for (int e = 0; e < 4; ++e) { const f32x2 v = d[i][e] * rs1 + off; ch[i][e] = cvtpk(v.x, v.y); }
  if (hjob) {
    #pragma unroll
    for (int i = 0; i < 8; ++i) ldsw(xa + i * 256, ch[i]);
    sync4(lb + L_CNT, 4, lane); spin_lds(lb + L_CNT, 4);
    #pragma unroll
    for (int i = 0; i < 8; ++i) {
      const int u = w * 8 + i, kgl = u >> 4, gp = u & 15;
      const unsigned ad = lb + (8 * kgl + (li >> 2)) * HROW + (2 * gp + (g >> 1)) * 64 + (li & 3) * 16 + (g & 1) * 8;
      st1(rs, cat(trread(ad), trread(ad + 4 * HROW)), lane * 16, WS_H + ((2 * s0 + kgl) * 1024 + gp * 64) * 16);
    }
    STAMP(0, 4, hjob);
    sync4(lb + L_CNT, 8, lane);
    WAIT("vmcnt(0)");
    if (sync4(lb + L_AUX, 4, lane)) { if (lane < 8) raise(ws, WS_FH + lane * 1024 + s0 * 8, next_tag(epoch)); sync4(lb + L_AUX, 5, lane); }
    STAMP(0, 5, hjob);
    return;
  }
  u32x4 A2[4][2];
  #pragma unroll
  for (int nl = 0; nl < 4; ++nl) {
    const u32x4 f0 = {ch[0][nl], ch[1][nl], ch[2][nl], ch[3][nl]}, f1 = {ch[4][nl], ch[5][nl], ch[6][nl], ch[7][nl]};
    #pragma unroll
    for (int h = 0; h < 2; ++h) {
      const f32x4 re = mm32(f1, F1[h][0][1], mm32(f0, F1[h][0][0], Z4)), im = mm32(f1, F1[h][1][1], mm32(f0, F1[h][1][0], Z4));
      const f32x4 tr = re * twc[h][nl] + im * tws[h][nl], ti = im * twc[h][nl] - re * tws[h][nl];
      #pragma unroll
      for (int r = 0; r < 4; ++r) A2[r][h][nl] = cvtpk(tr[r], ti[r]);
    }
  }
  #pragma unroll
  for (int h = 0; h < 2; ++h) {
    f32x4 re[4], im[4];
    #pragma unroll
    for (int r = 0; r < 4; ++r) { re[r] = mm32(A2[r][h], F2[0], Z4); im[r] = mm32(A2[r][h], F2[1], Z4); }
    #pragma unroll
    for (int rho = 0; rho < 4; ++rho)
      ldsw(lb + L_AUX + e1_off(32 * li + 16 * h + 4 * g + rho, w), (u32x4){cvtpk(re[0][rho], im[0][rho]), cvtpk(re[1][rho], im[1][rho]), cvtpk(re[2][rho], im[2][rho]), cvtpk(re[3][rho], im[3][rho])});
  }
  sync4(lb + L_CNT, 4, lane); spin_lds(lb + L_CNT, 4);
  const bool swp = (s0 >> 2) & 1;
  u32x4 Bf[8];
  #pragma unroll
  for (int i = 0; i < 8; ++i) { const int p = 16 * (4 * w + 2 * (i >> 2) + (li >> 3)) + 1 + 4 * (li & 3) + (i & 3); Bf[i] = ldsr(lb + L_AUX + e1_off((li & 4) ? 512 - p : p, g)); }
  __builtin_amdgcn_sched_barrier(0);
  sync4(lb + L_CNT, 8, lane);
  Bf[7] ^= (w == 3 && li == 11) ? 0x80000000u : 0u;
  #pragma unroll
  for (int bh = 0; bh < 2; ++bh) {
    f32x4 re[4], im[4];
    #pragma unroll
    for (int c = 0; c < 4; ++c) { re[c] = mm32(Fc[0], Bf[4 * bh + c], Z4); im[c] = mm32(Fc[1], Bf[4 * bh + c], Z4); }
    #pragma unroll
    for (int rho = 0; rho < 4; ++rho) {
      const u32x2 pr = {cvtpk(re[0][rho], re[1][rho]), cvtpk(re[2][rho], re[3][rho])}, pi = {cvtpk(im[0][rho], im[1][rho]), cvtpk(im[2][rho], im[3][rho])};
      xo[4 * bh + rho] = swp ? cat(pi, pr) : cat(pr, pi);
    }
  }
  if (w == 0) {
    const u32x4 B = ldsr(lb + L_AUX + e1_off(0, g)) ^ (li == 1 ? 0x80000000u : 0u);
    const f32x4 re = mm32(Fc[0], B, Z4), im = mm32(Fc[1], B, Z4);
    const u32x2 pr = {cvtpk(re[0], re[1]), cvtpk(re[2], re[3])}, pi = {cvtpk(im[0], im[1]), cvtpk(im[2], im[3])};
    xo[8] = swp ? cat(pi, pr) : cat(pr, pi);
  }
}
DI void rows_store(const u32x4 (&xo)[9], int s0, int w, int lane, rsrc_t rs) {
  const int li = lane & 15, g = lane >> 4, xsw = (s0 >> 3) & 7;
  const unsigned base = WS_X1 + (s0 ^ ((s0 >> 1) & 1)) * 128;
  #pragma unroll
  for (int i = 0; i < 8; ++i) st1(rs, xo[i], (64 * g + (li >> 3)) * 16384 + ((li & 7) ^ xsw) * 16, base + ((i & 3) * 16 + 4 * w + 2 * (i >> 2)) * 16384);
  if (w == 0 && li < 2) st1(rs, xo[8], ((li * 4 + g) ^ xsw) * 16, base + 256 * 16384);
}

DI void touch_w1(int l, const char* w1) { const int row = l / 56, rr = row & 63; touch((4 * (row >> 6) + (rr & 3) + 128 * (rr >> 2)) * 7168 + (l % 56) * 128, w1); }
DI void touch_w2(int l, const char* w2) { touch(((l >> 6) & 1) * (1792 * 4096) + ((l >> 7) * 64 + (l & 63)) * 128, w2); }
constexpr int W_LINES = 114688;
DI void touch_from(int st, int n, int sb, int lane, const char* w1, const char* w2) {
  const int l = st * 3584 + (n * 32 + sb) * 64;
  if (l < W_LINES) touch_w1(l + lane, w1); else touch_w2(min(l - W_LINES, W_LINES - 64) + lane, w2);
}

struct ColC { u32x4 Fa[2], Fai[2], Fbi[2], Fb[2]; f32x4 t2c, t2s; float t1c[2], t1s[2], phc, phs; };
DI void col_consts(ColC& K, int u, int chi, int lane) {
  const int li = lane & 15, g = lane >> 4;
  { float c[4], s[4];
    #pragma unroll
    for (int e = 0; e < 4; ++e) { cs<16>((4 * g + e) * li, c[e], s[e]); K.Fai[0][e] = cvtpk(c[e], -s[e]); K.Fai[1][e] = cvtpk(s[e], c[e]); float tc, ts; cs<128>((li >> 1) * (4 * g + e), tc, ts); K.t2c[e] = tc; K.t2s[e] = ts; }
    K.Fa[0] = (u32x4){cvtpk(c[0], c[1]), cvtpk(c[2], c[3]), cvtpk(s[0], s[1]), cvtpk(s[2], s[3])};
    K.Fa[1] = (u32x4){K.Fa[0][2] ^ 0x80008000u, K.Fa[0][3] ^ 0x80008000u, K.Fa[0][0], K.Fa[0][1]}; }
  #pragma unroll
  for (int lo = 0; lo < 2; ++lo) {
    float c, s; cs<8>((2 * g + lo) * (li >> 1), c, s);
    const unsigned u0 = cvtpk(c, -s), u1 = cvtpk(s, c);
    K.Fbi[0][lo] = (li & 1) ? 0u : u0; K.Fbi[0][2 + lo] = (li & 1) ? u0 : 0u; K.Fbi[1][lo] = (li & 1) ? 0u : u1; K.Fbi[1][2 + lo] = (li & 1) ? u1 : 0u;
    cs<8>((2 * g + lo) * (2 * (li >> 2) + (li & 1)), c, s); c *= 0x1p-21f; s *= 0x1p-21f;
    K.Fb[0][lo] = (li & 2) ? cvtpk(-s, c) : cvtpk(c, s); K.Fb[0][2 + lo] = 0u; K.Fb[1][lo] = 0u; K.Fb[1][2 + lo] = K.Fb[0][lo];
    cs<128>((2 * g + lo) * li, K.t1c[lo], K.t1s[lo]);
  }
  if (u & 64) { const u32x4 t = K.Fai[0]; K.Fai[0] = K.Fai[1]; K.Fai[1] = t; }
  cs<1024>(u < 256 ? 16 * (u & 15) + 1 + 4 * chi + (lane & 3) : 0, K.phc, K.phs);
  #pragma unroll
  for (int h = 0; h < 2; ++h) asm volatile("" : "+v"(K.Fa[h]), "+v"(K.Fai[h]), "+v"(K.Fbi[h]), "+v"(K.Fb[h]), "+v"(K.t1c[h]), "+v"(K.t1s[h]));
  asm volatile("" : "+v"(K.phc), "+v"(K.phs), "+v"(K.t2c), "+v"(K.t2s));
}
DI void col_stage(int u, int q, unsigned buf, int lane, const char* ws) {
  #pragma unroll
  for (int i = 0; i < 8; ++i) glds1(lane * 16, ws + (i < 4 ? WS_X1 : WS_WT) + (size_t)u * 16384 + q * 4096 + (i & 3) * 1024, buf + (i >> 2) * 16384 + q * 4096 + (i & 3) * 1024);
}
template <class Sync>
DI void col_run(const ColC& K, int u, int chi, unsigned buf, int lane, rsrc_t rs, Sync sync) {
  const int li = lane & 15, g = lane >> 4, sl = 32 * g + 8 * (li >> 2) + 2 * (li & 3);
  const unsigned L = buf + (((sl ^ ((sl >> 1) & 1)) * 128 + ((sl >> 3) & 7) * 16 + ((sl >> 2) & 1) * 8) ^ (chi * 16));
  u32x4 Af[4], B2[4], wv[4];
  #pragma unroll
  for (int i = 0; i < 4; ++i) { const unsigned ad = L ^ ((i & 1) * 128 + (i >> 1) * 64); Af[i] = cat(trread(ad), trread(ad ^ 8)); }
  #pragma unroll
  for (int i = 0; i < 4; ++i) wv[i] = ldsr(buf + 16384 + (i >> 1) * 8192 + lane * 128 + (((i & 1) * 4 + chi) ^ ((lane >> 1) & 7)) * 16);
  asm volatile("s_waitcnt lgkmcnt(0)" : "+v"(Af[0]), "+v"(Af[1]), "+v"(Af[2]), "+v"(Af[3]), "+v"(wv[0]), "+v"(wv[1]), "+v"(wv[2]), "+v"(wv[3]) :: "memory");
  sync();
  #pragma unroll
  for (int st = 0; st < 2; ++st)
    #pragma unroll
    for (int lo = 0; lo < 2; ++lo) {
      const f32x4 re = mm32(Af[2 * st + lo], K.Fa[0], Z4), im = mm32(Af[2 * st + lo], K.Fa[1], Z4);
      const f32x4 tr = re * K.t1c[lo] + im * K.t1s[lo], ti = im * K.t1c[lo] - re * K.t1s[lo];
      #pragma unroll
      for (int cl = 0; cl < 4; ++cl) B2[cl][2 * st + lo] = cvtpk(tr[cl], ti[cl]);
    }
  f32x4 ore[4], oim[4];
  #pragma unroll
  for (int cl = 0; cl < 4; ++cl) {
    const f32x4 da = mm32(K.Fb[0], B2[cl], Z4), db = mm32(K.Fb[1], B2[cl], Z4);
    const float cp = __builtin_bit_cast(float, __builtin_amdgcn_readlane(__builtin_bit_cast(int, K.phc), cl));
    const float sp = __builtin_bit_cast(float, __builtin_amdgcn_readlane(__builtin_bit_cast(int, K.phs), cl));
    #define WPAIR(K, D) ((cl & 1) ? (f32x2){bfhi(wv[K][D]), bfhi(wv[2 + K][D])} : (f32x2){bflo(wv[K][D]), bflo(wv[2 + K][D])})
    const f32x2 w1r = WPAIR(0, cl >> 1), w1i = WPAIR(0, 2 + (cl >> 1)), w2r = WPAIR(1, cl >> 1), w2n = WPAIR(1, 2 + (cl >> 1));
    #undef WPAIR
    const f32x2 sr = w1r + w2r, si = w1i - w2n, dr = w1r - w2r, di = w1i + w2n;
    const f32x2 alr = sr - sp * dr, ali = si - sp * di, der = sr + sp * dr, dei = si + sp * di, ben = cp * di, bei = cp * dr;
    const f32x2 ar = {da[0], da[1]}, ai = {da[2], da[3]}, br = {db[0], db[1]}, bi = {db[2], db[3]};
    const f32x2 par = alr * ar - ali * ai - ben * br - bei * bi, pai = alr * ai + ali * ar - ben * bi + bei * br;
    const f32x2 pbr = der * br - dei * bi + ben * ar + bei * ai, pbi = der * bi + dei * br + ben * ai - bei * ar;
    const u32x4 fr = {cvtpk(par.x, pai.x), cvtpk(par.y, pai.y), cvtpk(pbr.x, pbi.x), cvtpk(pbr.y, pbi.y)};
    const f32x4 re = mm32(fr, K.Fbi[0], Z4), im = mm32(fr, K.Fbi[1], Z4);
    const f32x4 ur = re * K.t2c - im * K.t2s, ui = re * K.t2s + im * K.t2c;
    const u32x4 f2 = {cvtpk(ur[0], ui[0]), cvtpk(ur[1], ui[1]), cvtpk(ur[2], ui[2]), cvtpk(ur[3], ui[3])};
    ore[cl] = mm32(K.Fai[0], f2, Z4); oim[cl] = mm32(K.Fai[1], f2, Z4);
  }
const unsigned zo = buf + (li >> 1) * 128 + g * 4096 + (((((li ^ (u >> 5)) & 1) * 4 + chi) ^ (li >> 1)) * 16);
  #pragma unroll
  for (int rg = 0; rg < 4; ++rg)
    ldsw(zo + rg * 1024, (u32x4){cvtpk(ore[0][rg], ore[1][rg]), cvtpk(ore[2][rg], ore[3][rg]), cvtpk(oim[0][rg], oim[1][rg]), cvtpk(oim[2][rg], oim[3][rg])});
  WAIT("lgkmcnt(0)");
  sync();
  #pragma unroll
  for (int i = 0; i < 4; ++i) st1(rs, ldsr(buf + chi * 4096 + i * 1024 + (lane >> 3) * 128 + (((lane & 7) ^ (lane >> 3)) * 16)), lane * 16 + i * 1024, WS_Z1 + u * 16384 + chi * 4096);
}

DI unsigned inv_eoff(int k, int pos) { const int k1 = k >> 5; return (k * 32 + pos * 8) ^ (((k1 ^ (k >> 2)) & 3) * 8) ^ (((k1 >> 2) & 1) * 128); }
template <int DEN> DI void inv_planar(u32x4 (&G)[2], int rowf, int g) {
  float c[4], s[4];
  #pragma unroll
  for (int e = 0; e < 4; ++e) cs<DEN>(rowf * (4 * g + e), c[e], s[e]);
  G[1] = (u32x4){cvtpk(s[0], s[1]), cvtpk(s[2], s[3]), cvtpk(c[0], c[1]), cvtpk(c[2], c[3])};
  G[0] = (u32x4){G[1][2], G[1][3], G[1][0] ^ 0x80008000u, G[1][1] ^ 0x80008000u};
  asm volatile("" : "+v"(G[0]), "+v"(G[1]));
}
DI void inv_consts(u32x4 (&U)[9], int lane) {
  const int li = lane & 15, g = lane >> 4;
  u32x4 G2[2];
  inv_planar<16>(G2, li, g);
  U[0] = G2[0]; U[1] = G2[1];
  #pragma unroll
  for (int u = 0; u < 8; ++u) { float c, s; cs<32>((4 * u + g) * li, c, s); U[2 + (u >> 2)][u & 3] = cvtpk(c, -s); U[4 + (u >> 2)][u & 3] = cvtpk(s, c); }
}
DI unsigned inv_img(unsigned lb, int w) { return lb + (w >> 1) * L_T + (w & 1) * 8192; }
DI void inv_stage(u32x4& sp, int s0, int w, int lane, unsigned lb, const char* ws) {
  ld1_async(sp, (lane >> 4) * 16, ws + WS_Z1 + 256 * 16384 + s0 * 128);
  #pragma unroll
  for (int i = 0; i < 8; ++i) glds1((lane >> 3) * 262144 + (lane & 7) * 16, ws + WS_Z1 + (size_t)((i & 1) * 128 + 4 * w + (i >> 1)) * 16384 + s0 * 128, inv_img(lb, w) + i * 1024);
}
template <class Mid>
DI void inv_cols(const u32x4 (&Gc)[2], u32x4 sp, int w, int lane, unsigned lb, Mid mid) {
  const int li = lane & 15, g = lane >> 4, j2 = 4 * g + (li >> 2);
  const unsigned L = j2 * 128 + ((j2 >> 1) & 1) * 64 + (li & 3) * 16 + ((j2 >> 2) & 1) * 8;
  u32x4 Bf[8];
  #pragma unroll
  for (int i = 0; i < 8; ++i) { const unsigned ad = (i >> 1) * 2048 + (L ^ ((i & 1) * 64)); Bf[i] = cat(trread(inv_img(lb, w) + ad), trread(inv_img(lb, w) + (ad ^ 8))); }
  __builtin_amdgcn_sched_barrier(0);
  #pragma unroll
  for (int i = 0; i < 8; ++i) {
    if (i == 3) { asm volatile("s_waitcnt lgkmcnt(0)" : "+v"(Bf[3]), "+v"(Bf[4]), "+v"(Bf[5]), "+v"(Bf[6]), "+v"(Bf[7]) :: "memory"); mid(); }
    const int st = i & 1, p = 16 * (4 * w + (i >> 1)) + 1 + li;
    const f32x4 re = mm32(Gc[0], Bf[i], Z4), im = mm32(Gc[1], Bf[i], Z4);
    const unsigned eo = lb + L_AUX + inv_eoff(st ? 512 - p : p, g), sg = st ? 0x80008000u : 0u;
    if (i < 7 || p != 256) {
      *(LAS u32x2*)(size_t)eo = (u32x2){cvtpk(re[0], re[1]), cvtpk(re[2], re[3])};
      *(LAS u32x2*)(size_t)(eo + 16384) = (u32x2){cvtpk(im[0], im[1]) ^ sg, cvtpk(im[2], im[3]) ^ sg};
    }
  }
  if (w == 0) {
    const f32x4 re = mm32(Gc[0], sp, Z4), im = mm32(Gc[1], sp, Z4);
    if (li == 0) {
      const unsigned eo = lb + L_AUX + inv_eoff(0, g);
      *(LAS u32x2*)(size_t)eo = (u32x2){cvtpk(re[0], re[1]), cvtpk(re[2], re[3])};
      *(LAS u32x2*)(size_t)(eo + 16384) = (u32x2){cvtpk(im[0], im[1]), cvtpk(im[2], im[3])};
    }
  }
}
template <class Mid>
DI void inv_rows1(const u32x4 (&U)[9], u32x4 (&A1)[4][2], int w, int lane, unsigned lb, Mid mid) {
  const int li = lane & 15, g = lane >> 4;
  u32x4 Bf[8];
  #pragma unroll
  for (int u = 0; u < 8; ++u) { const unsigned ad = lb + L_AUX + inv_eoff(32 * (4 * g + (li >> 2)) + 4 * u + (li & 3), w); Bf[u] = cat(trread(ad), trread(ad + 16384)); }
  __builtin_amdgcn_sched_barrier(0);
  #pragma unroll
  for (int u = 0; u < 8; ++u) {
    if (u == 4) { asm volatile("" : "+v"(A1[0][0]), "+v"(A1[1][0]), "+v"(A1[2][0]), "+v"(A1[3][0]), "+v"(Bf[4])); mid(); asm volatile("" : "+v"(Bf[4])); }
    const f32x4 re = mm32(Bf[u], U[0], Z4), im = mm32(Bf[u], U[1], Z4);
    float tc, ts; cs<512>((4 * u + g) * li, tc, ts);
    const f32x4 tr = re * tc - im * ts, ti = re * ts + im * tc;
    #pragma unroll
    for (int r = 0; r < 4; ++r) A1[r][u >> 2][u & 3] = cvtpk(tr[r], ti[r]);
  }
  asm volatile("" : "+v"(A1[0][0]), "+v"(A1[0][1]), "+v"(A1[1][0]), "+v"(A1[1][1]), "+v"(A1[2][0]), "+v"(A1[2][1]), "+v"(A1[3][0]), "+v"(A1[3][1]));
}
DI void inv_rows2(const u32x4 (&U)[9], const u32x4 (&A1)[4][2], int s0, int w, int lane, rsrc_t rs) {
  #pragma unroll
  for (int h = 0; h < 2; ++h) {
    const unsigned sg = (h && (lane & 16)) ? 0x80008000u : 0u;
    const u32x4 G0 = U[2] ^ sg, G1 = U[3] ^ sg, G2 = U[4] ^ sg, G3 = U[5] ^ sg;
    #pragma unroll
    for (int r = 0; r < 4; ++r) {
      const f32x4 re = mm32(A1[r][1], G1, mm32(A1[r][0], G0, Z4)), im = mm32(A1[r][1], G3, mm32(A1[r][0], G2, Z4));
      st1(rs, (u32x4){cvtpk(re[0], im[0]), cvtpk(re[1], im[1]), cvtpk(re[2], im[2]), cvtpk(re[3], im[3])}, lane * 16, WS_R + (s0 + 128 * (4 * w + r)) * 2048 + h * 1024);
    }
  }
}

DI void spare_units(int bx, int w, int lane, unsigned lb, char* ws, rsrc_t rs, u64 tag) {
  unsigned n = 0;
  auto sync = [&]() { n += 4; sync4(lb + L_CNT2, n, lane); spin_lds(lb + L_CNT2, n); };
  if (w == 0) wait_flags<128, 16>(rs, WS_FA + (bx & 7) * 2048, lane, tag);
  sync();
  if (bx == 224 && w == 0 && lane == 0) raise(ws, WS_EP, tag);
  #pragma unroll 1
  for (int u = bx; u <= 256; u += 32) {
    ColC K;
    col_stage(u, w, lb, lane, ws);
    col_consts(K, u, w, lane);
    WAIT("vmcnt(0)");
    sync();
    col_run(K, u, w, lb, lane, rs, sync);
    WAIT("vmcnt(0)");
    sync();
  }
  if (w == 0) { if (lane == 0) raise(ws, WS_FZ + bx * 8, tag); *(volatile LAS unsigned*)(size_t)(lb + L_HOLD) = 1u; }
  STAMP(3, 2, 0);
}

template <int NT> struct Ring {
  const char *P, *T; unsigned tstep, lb, pv[4], tv[4]; int cls;
  DI int stage(int t) const { const int ph = (t + LEAD - cls) & 3, lead = t + LEAD - ph; return (ph > LEAD || lead < 4 || lead >= NT) ? t : (ph == 0 ? lead : t - 1); }
  DI void issueP(int t) const {
    const char* pb = P + (size_t)stage(t) * 131072;
    #pragma unroll
    for (int i = 0; i < 4; ++i) glds1(pv[i], pb, lb + (t & 3) * 16384 + i * 1024);
  }
  DI void issueT(int t) const {
    const char* tb = T + (size_t)stage(t) * tstep;
    #pragma unroll
    for (int i = 0; i < 4; ++i) glds(tv[i], tb, lb + L_T + (t & 3) * 16384 + i * 1024);
  }
  template <int NP> DI void start(char* ws, rsrc_t rs, unsigned off, int lane, u64 tag) const {
    issueP(0); issueP(1); issueP(2); issueP(3);
    WAIT_BAR("vmcnt(12)");
    WAIT_BAR("vmcnt(8)");
    u32x4 f;
    ld1_async(f, (lane < NP ? lane : 0) * 16, ws + off);
    issueT(4); issueP(4);
    WAIT_BAR("vmcnt(13)"); issueT(5); issueP(5);
    asm volatile("s_waitcnt vmcnt(16)\n\ts_barrier" : "+v"(f) :: "memory");
    if (!__all(both(f, tag))) wait_flags<NP>(rs, off, lane, tag);
    issueT(6); issueP(6);
  }
};
#define RING_STEPS(R, T0, T1) _Pragma("unroll 1") for (int t = (T0); t < (T1); ++t) { WAIT_BAR("vmcnt(16)"); LSTAMP(3 + t); \
  if (RETOUCH && lw == 0 && (t & (RETOUCH - 1)) == 2) touch(codeoff, (const char*)code); (R).issueT(t + 4); (R).issueP(t + 4); }

#define KLOOP_ADDR(NX) \
  const int kq = li >> 2, kx = (4 * g + kq) & 7; \
  const unsigned pa = lb + g * 2048 + (wr * 64 + li) * 16, tb = lb + L_T + (4 * g + kq) * 256 + (lane & 3) * 8; \
  const unsigned ta0 = tb + (((4 * wc + (0 ^ NX)) ^ kx) * 32), ta1 = tb + (((4 * wc + (1 ^ NX)) ^ kx) * 32), ta2 = tb + (((4 * wc + (2 ^ NX)) ^ kx) * 32), ta3 = tb + (((4 * wc + (3 ^ NX)) ^ kx) * 32); \
  f32x16 a0 = {}, a1 = {}, a2 = {}, a3 = {}; unsigned cnt
#define KLOOP_ACC "+{v[64:79]}"(a0), "+{v[80:95]}"(a1), "+{v[96:111]}"(a2), "+{v[112:127]}"(a3)
#define KLOOP_RUN(TXT, NQ) asm volatile(TXT : KLOOP_ACC, [cnt] "=&s"(cnt) : [pa] "v"(pa), [ta0] "v"(ta0), [ta1] "v"(ta1), [ta2] "v"(ta2), [ta3] "v"(ta3), [nq] "i"(NQ) : "memory", "scc", KLOOP_CLOB)

DI f32x2 gelu2(f32x2 u) {
  const f32x2 z = u * (u * u * -0.1029432397f + -2.3022081985f);
  f32x2 e; e.x = __builtin_amdgcn_exp2f(z.x); e.y = __builtin_amdgcn_exp2f(z.y);
  e += 1.0f;
  f32x2 r; r.x = __builtin_amdgcn_rcpf(e.x); r.y = __builtin_amdgcn_rcpf(e.y);
  return u * r;
}

__global__ __launch_bounds__(512, 2) void k_fused(const char* __restrict__ x, const unsigned short* __restrict__ wr_, const unsigned short* __restrict__ wi_, const char* __restrict__ w1,
                                                  const unsigned short* __restrict__ b1, const char* __restrict__ w2, const unsigned short* __restrict__ b2, char* ws, char* __restrict__ out) {
  extern __shared__ __attribute__((aligned(16))) char smem[];
  u64 code;
  asm volatile("s_getpc_b64 %0" : "=s"(code));
  const unsigned codeoff = min((((blockIdx.x >> 3) & 15) * 27 + min(threadIdx.x & 63, 26u)) * 128, (unsigned)CODE_BYTES);
  if (CODE_BYTES && threadIdx.x < 64) touch(codeoff, (const char*)code);
  asm volatile("" :: "s"(x), "s"(wr_), "s"(wi_), "s"(w1), "s"(b1), "s"(w2), "s"(b2), "s"(ws), "s"(out));
  const int tid = threadIdx.x, lane = tid & 63, wid = __builtin_amdgcn_readfirstlane(tid >> 6), g = lane >> 4, li = lane & 15;
  const int bx = blockIdx.x, xcd = bx & 7, r = bx >> 3;
  const unsigned lb = (unsigned)(size_t)smem;
  const rsrc_t rs = __builtin_amdgcn_make_buffer_rsrc(ws, 0, WS_END, 0x00020000);
  const u32x2 epoch = __builtin_amdgcn_raw_buffer_load_b64(rs, 0, WS_EP, 16);
  const int rq = r >> 2, nh = rq < 5 ? 2 : (rq < 7 ? 3 : 0), hb4 = 2 * rq + (rq > 5 ? rq - 5 : 0);
  const bool spare = r >= 28, hjob = (r & 3) < nh;
  const int s0 = (hjob ? hb4 + (r & 3) : (spare ? r - 16 : 4 * rq - hb4 + (r & 3) - nh)) * 8 + xcd;
  const bool ijob = !spare && (!hjob || s0 < 32);
  const int si = hjob ? s0 + 96 : s0;
  const int dt1 = (xcd & 1) * 4 + (r & 3), ft = (xcd >> 1) * 7 + (r >> 2), cls = ((r & 3) + (r >> 2)) & 3;
  const int kh = xcd & 1, dt2 = ((xcd >> 1) & 1) * 4 + (r & 3), stl = (xcd >> 2) * 8 + (r >> 2);
  STAMP(0, 6, hjob);

  if (wid >= 4) {
    const int lw = wid - 4;
    const unsigned go = lb + 49152 + lw * 4096;
    *(volatile LAS unsigned*)(size_t)go = 0u;
    if (lw == 0) { *(volatile LAS unsigned*)(size_t)(lb + L_CNT) = 0u; *(volatile LAS unsigned*)(size_t)(lb + L_CNT2) = 0u; *(volatile LAS unsigned*)(size_t)(lb + L_HOLD) = 0u; *(volatile LAS unsigned*)(size_t)(lb + L_AUX) = 0u; }
    WAIT_BAR("lgkmcnt(0)");
    u32x4 Gc[2];
    inv_planar<2048>(Gc, si + 128 * li, g);
    Ring<28> R2;
    R2.P = ws + WS_G + (size_t)kh * 28 * 131072; R2.T = w2 + (size_t)kh * 28 * 64 * 4096; R2.tstep = 64 * 4096; R2.lb = lb + lw * 4096; R2.cls = cls;
    #pragma unroll
    for (int i = 0; i < 4; ++i) {
      const int row = lw * 16 + i * 4 + g;
      R2.pv[i] = ((2 * lw + (i >> 1)) * 1024 + dt2 * 128 + (i & 1) * 64 + lane) * 16;
      R2.tv[i] = row * 4096 + stl * 256 + (li ^ ((row & 7) << 1)) * 16;
    }
    if (LDELAY) asm volatile("s_sleep %0" :: "i"(LDELAY));
    Retile T;
    retile_load(T, bx * 4 + lw, lane, wr_, wi_);
    Ring<32> R1;
    R1.P = ws + WS_H; R1.T = w1; R1.tstep = 4 * 7168; R1.lb = lb + lw * 4096; R1.cls = cls;
    #pragma unroll
    for (int i = 0; i < 4; ++i) {
      const int row = lw * 16 + i * 4 + g, kg = 4 * (row >> 5) + ((row >> 2) & 3), j = 4 * ((row >> 4) & 1) + (row & 3);
      R1.pv[i] = ((2 * lw + (i >> 1)) * 1024 + dt1 * 128 + (i & 1) * 64 + lane) * 16;
      R1.tv[i] = ((kg >> 1) + 128 * (8 * (kg & 1) + j)) * 7168 + ft * 256 + (li ^ ((row & 7) << 1)) * 16;
    }
    auto pass = [&](unsigned word, unsigned target, unsigned state) {
      if (lw == 0) { spin_lds(word, target); if (lane < 4) *(volatile LAS unsigned*)(size_t)(lb + 49152 + lane * 4096) = state; }
      else spin_lds(go, state);
    };
    if (hjob && WLATE) { pass(lb + L_CNT, 8, 1); STAMP(4, 1, hjob); R1.issueT(0); pass(lb + L_AUX, 5, 2); WAIT("vmcnt(4)"); }
    else if (!hjob && !spare) { R1.issueT(0); R1.issueT(1); R1.issueT(2); R1.issueT(3); WAIT("vmcnt(16)"); }
    else WAIT("vmcnt(0)");
    retile_store(T, bx * 4 + lw, lane, go + 2048, rs);
    u64 tag;
    if (spare) {
      #pragma unroll 1
      for (int n = 0; n < PF1; ++n) touch_from(4, n * 4 + lw, bx - 224, lane, w1, w2);
      tag = next_tag(epoch);
      WAIT_BAR("vmcnt(0)");
      #pragma unroll 1
      for (int n = 0; n < PF2; ++n) { touch_from(ST2, n * 4 + lw, bx - 224, lane, w1, w2); WAIT("vmcnt(1)"); }
      spin_lds(lb + L_HOLD, 1);
      #pragma unroll 1
      for (int n = 0; n < PF3; ++n) {
        u32x2 f;
        asm volatile("global_load_dwordx2 %0, %1, %2 sc1" : "=&v"(f) : "v"(0u), "s"(ws + WS_FS + xcd * 256) : "memory");
        touch_from(ST3, n * 4 + lw, bx - 224, lane, w1, w2);
        asm volatile("s_waitcnt vmcnt(1)" : "+v"(f) :: "memory");
        if (__builtin_amdgcn_readfirstlane(f.x) == (unsigned)tag && __builtin_amdgcn_readfirstlane(f.y) == (unsigned)(tag >> 32)) break;
      }
      STAMP(3, 4, 0);
      R2.issueT(0); R2.issueT(1); R2.issueT(2); R2.issueT(3);
    } else {
      if (hjob) { if (WLATE) { R1.issueT(1); R1.issueT(2); R1.issueT(3); } else { pass(lb + L_CNT, 8, 1); STAMP(4, 1, hjob); R1.issueT(0); pass(lb + L_AUX, 5, 2); R1.issueT(1); R1.issueT(2); R1.issueT(3); } }
      tag = next_tag(epoch);
      STAMP(4, 2, hjob);
      if (lw == 0) {
        wait_flags<FIRST ? 16 : 64>(rs, WS_FH + xcd * 1024, lane, tag);
        spin_lds(lb + L_CNT, 8);
        if (lane < 4) *(volatile LAS unsigned*)(size_t)(lb + 49152 + lane * 4096) = 3u;
        WAIT("lgkmcnt(0)");
      } else {
        spin_lds(go, 3);
        WAIT("vmcnt(0)");
      }
      STAMP(4, 3, hjob);
      LSTAMP(0);
      R1.start<64>(ws, rs, WS_FH + xcd * 1024, lane, tag);
      u32x4 fa0, fa1;
      ld1_async(fa0, lane * 16, ws + WS_FA + xcd * 2048); ld1_async(fa1, lane * 16, ws + WS_FA + xcd * 2048 + 1024);
      RING_STEPS(R1, 3, 5);
      WAIT_BAR("vmcnt(16)");
      asm volatile("" : "+v"(fa0), "+v"(fa1));
      if (!__all(both(fa0, tag) && both(fa1, tag))) wait_flags<128>(rs, WS_FA + xcd * 2048, lane, tag);
      STAMP(3, 3, 0);
      col_stage(bx, lw, lb + L_AUX, lane, ws);
      R1.issueT(9); R1.issueP(9);
      WAIT_BAR("vmcnt(24)"); R1.issueT(10); R1.issueP(10);
      WAIT_BAR("vmcnt(16)"); BAR(); R1.issueT(11); R1.issueP(11); BAR();
      RING_STEPS(R1, 8, 22);
      u32x4 fz0, fz1;
      ld1_async(fz0, lane * 16, ws + WS_FZ); ld1_async(fz1, lane * 16, ws + WS_FZ + 1024);
      RING_STEPS(R1, 22, 28);
      WAIT_BAR("vmcnt(16)");
      asm volatile("" : "+v"(fz0), "+v"(fz1));
      if (r == 0 && lw == 0 && lane == 0) raise(ws, WS_FS + xcd * 256, tag);
      LSTAMP(31); LDUMP(0);
      if (ijob) {
        if (!__all(both(fz0, tag) && both(fz1, tag))) wait_flags<128>(rs, WS_FZ, lane, tag);
        STAMP(4, 4, hjob);
        u32x4 sp;
        inv_stage(sp, si, lw, lane, lb, ws);
        WAIT_BAR("vmcnt(17)"); R2.issueT(1);
        WAIT_BAR("vmcnt(13)"); R2.issueT(2);
        asm volatile("s_waitcnt vmcnt(8)" : "+v"(sp) :: "memory");
        inv_cols(Gc, sp, lw, lane, lb, [&]() { BAR(); R2.issueT(0); R2.issueT(3); });
        STAMP(4, 5, hjob);
        WAIT_BAR("lgkmcnt(0)");
      } else {
        R2.issueT(0);
        WAIT_BAR("vmcnt(12)"); R2.issueT(1);
        WAIT_BAR("vmcnt(8)"); R2.issueT(2);
        BAR();
        R2.issueT(3);
      }
    }
    if (lw == 0) wait_flags<FIRST ? 8 : 28>(rs, WS_FG + (dt2 * 28 + kh * 14) * 32, lane, tag);
    STAMP(4, 6, hjob);
    BAR();
    LSTAMP(0);
    R2.start<28>(ws, rs, WS_FG + (dt2 * 28 + kh * 14) * 32, lane, tag);
    RING_STEPS(R2, 3, 16);
    u32x4 fr0, fr1;
    ld1_async(fr0, lane * 16, ws + WS_FR); ld1_async(fr1, lane * 16, ws + WS_FR + 1024);
    RING_STEPS(R2, 16, 24);
    asm volatile("" : "+v"(fr0), "+v"(fr1));
    if (!__all(both(fr0, tag) && both(fr1, tag))) wait_flags<128>(rs, WS_FR, lane, tag);
    const size_t ro = (size_t)(stl * 128 + (lw >> 1) * 64 + kh * 32 + (lw & 1) * 16) * 2048;
    #pragma unroll
    for (int i = 0; i < 4; ++i) {
      const int pc = li ^ (4 * i + g);
      glds((4 * i + g) * 2048 + pc * 16, x + ro + dt2 * 256, lb + L_AUX + lw * 4096 + i * 1024);
      glds1((4 * i + g) * 2048 + (pc >> 2) * 256 + (pc & 3) * 16, ws + WS_R + ro + (dt2 >> 2) * 1024 + (dt2 & 3) * 64, lb + L_AUX + 16384 + lw * 4096 + i * 1024);
    }
    WAIT_BAR("vmcnt(24)"); WAIT_BAR("vmcnt(16)"); WAIT_BAR("vmcnt(8)"); WAIT_BAR("vmcnt(0)");
    LSTAMP(27); LDUMP(1);
    return;
  }

  const int wr = wid >> 1, wc = wid & 1;
  u32x4 xo[9] = {};
  rows_job(xo, hjob, s0, wid, lane, lb, x, rs, ws, epoch);
  const u64 tag = next_tag(epoch);
  STAMP(0, 2, hjob);
  if (!hjob) rows_store(xo, s0, wid, lane, rs);
  if (spare) {
    const int ln = fresh(lane);
    WAIT_BAR("vmcnt(0)");
    if (wid == 0 && lane < 8) raise(ws, WS_FA + lane * 2048 + bx * 8, tag);
    spare_units(bx, wid, ln, lb, ws, rs, tag);
  }
  int rr = r;
  asm volatile("" : "+s"(rr));
  if (rr < 28) {
    ColC K;
    col_consts(K, bx, wid, lane);
    u32x4 U[9];
    if (ijob) inv_consts(U, lane);
    asm volatile("" : "+v"(U[0]), "+v"(U[1]), "+v"(U[2]), "+v"(U[3]), "+v"(U[4]), "+v"(U[5]));
    KLOOP_ADDR(0);
    #pragma unroll
    for (int n = 0; n < 4; ++n) {
      const u32x2 bw = *(const u32x2*)(b1 + ft * 128 + wc * 64 + n * 16 + g * 4);
      f32x16& an = n == 0 ? a0 : n == 1 ? a1 : n == 2 ? a2 : a3;
      #pragma unroll
      for (int m = 0; m < 4; ++m) { an[4 * m] = bflo(bw.x); an[4 * m + 1] = bfhi(bw.x); an[4 * m + 2] = bflo(bw.y); an[4 * m + 3] = bfhi(bw.y); }
    }
    STAMP(0, 3, hjob);
    asm volatile("s_waitcnt vmcnt(0)\n\ts_barrier" : KLOOP_ACC :: "memory");
    if (wid == 0 && lane < 8) raise(ws, WS_FA + lane * 2048 + bx * 8, tag);
    STAMP(1, 0, hjob);
    unsigned tmp; const u64 tv = tag;
#define KLOOP_SIG(NQ, OFF) asm volatile(KLOOP_TS : KLOOP_ACC, [cnt] "=&s"(cnt), [tmp] "=&s"(tmp) : [pa] "v"(pa), [ta0] "v"(ta0), [ta1] "v"(ta1), [ta2] "v"(ta2), [ta3] "v"(ta3), [nq] "i"(NQ), \
                   [zero] "v"(OFF), [one] "v"(tv), [sig] "s"(ws), [wid] "s"(wid) : "memory", "scc", KLOOP_CLOB)
    KLOOP_RUN(KLOOP_TA, 2);
    BAR();
    STAMP(3, 0, 0);
    col_run(K, bx, wid, lb + L_AUX, fresh(lane), rs, []() { BAR(); });
    STAMP(3, 1, 0);
    KLOOP_SIG(6, WS_FZ + bx * 8);
    STAMP(1, 1, hjob);
    BAR();
    const int ln = fresh(lane);
    #pragma unroll
    for (int mm = 0; mm < 2; ++mm) {
      const f32x16& lo = mm ? a2 : a0; const f32x16& hi = mm ? a3 : a1;
      #pragma unroll
      for (int m = 0; m < 4; ++m) {
        const f32x2 u0 = gelu2((f32x2){lo[4 * m], lo[4 * m + 1]}), u1 = gelu2((f32x2){lo[4 * m + 2], lo[4 * m + 3]});
        const f32x2 u2 = gelu2((f32x2){hi[4 * m], hi[4 * m + 1]}), u3 = gelu2((f32x2){hi[4 * m + 2], hi[4 * m + 3]});
        st1(rs, (u32x4){cvtpk(u0.x, u0.y), cvtpk(u1.x, u1.y), cvtpk(u2.x, u2.y), cvtpk(u3.x, u3.y)}, (ln >> 4) * 16384 + (ln & 15) * 16 + m * 256, WS_G + ((ft * 16 + wc * 8 + mm * 4) * 1024 + dt1 * 128 + wr * 64) * 16);
      }
    }
    STAMP(1, 2, hjob);
    asm volatile("" ::: "memory");
    u32x4 A1[4][2];
    auto publish = [&]() { WAIT("vmcnt(0)"); if (lane == 0) raise(ws, WS_FG + ((dt1 * 28 + ft) * 4 + wid) * 8, tag); STAMP(1, 3, hjob); };
    if (!ijob) publish();
    else { BAR(); inv_rows1(U, A1, wid, ln, lb, publish); inv_rows2(U, A1, si, wid, ln, rs); }
  }
  {
  const int lane = fresh(tid & 63), g = lane >> 4, li = lane & 15;
  if (!ijob) {
    #pragma unroll
    for (int i = 0; i < 8; ++i) st1(rs, (u32x4){EMPTY, EMPTY, EMPTY, EMPTY}, lane * 16 + i * 1024, WS_XC + ((spare ? bx - 128 : s0 - 32) * 4 + wid) * 8192);
  }
  const int srow = stl * 128 + wc * 64 + kh * 32;
  const unsigned short bb0 = b2[srow + li], bb1 = b2[srow + 16 + li];
  KLOOP_ADDR((2 * kh));
  STAMP(1, 4, hjob);
  asm volatile("s_barrier\n\ts_waitcnt vmcnt(0)\n\ts_barrier" : KLOOP_ACC :: "memory");
  if (wid == 0 && lane == 0) raise(ws, WS_FR + bx * 8, tag);
  STAMP(2, 0, hjob);
  KLOOP_RUN(KLOOP_PA, 7);
  STAMP(2, 1, hjob);
  #pragma unroll
  for (int i = 0; i < 4; ++i) {
    const f32x16& s = i < 2 ? a2 : a3; const int o = 8 * (i & 1);
    st1(rs, (u32x4){min(cvtpk(s[o], s[o + 1]), EMPTY - 1), min(cvtpk(s[o + 2], s[o + 3]), EMPTY - 1), min(cvtpk(s[o + 4], s[o + 5]), EMPTY - 1), min(cvtpk(s[o + 6], s[o + 7]), EMPTY - 1)}, lane * 16 + i * 1024, WS_XC + ((bx ^ 1) * 4 + wid) * 4096);
  }
  BAR();
  f32x2 own[4][4];
  #pragma unroll
  for (int i = 0; i < 4; ++i) {
    const f32x16& s = i < 2 ? a0 : a1; const int o = 8 * (i & 1), n = i >> 1, m = i & 1;
    const unsigned rowa = lb + L_AUX + (wc * 32 + n * 16 + li) * 256;
    const u32x4 xv = ldsr(rowa + ((wr * 8 + m * 4 + g) ^ li) * 16), rv = ldsr(rowa + 16384 + ((g * 4 + wr * 2 + m) ^ li) * 16);
    const float bv = bflo(n ? bb1 : bb0);
    #pragma unroll
    for (int e = 0; e < 4; ++e) own[i][e] = (f32x2){s[o + 2 * e], s[o + 2 * e + 1]} + bv + ((f32x2){bflo(xv[e]), bfhi(xv[e])} + (f32x2){bflo(rv[e]), bfhi(rv[e])});
    asm volatile("" : "+v"(own[i][0]), "+v"(own[i][1]), "+v"(own[i][2]), "+v"(own[i][3]));
  }
  STAMP(2, 2, hjob);
  u32x4 rx[4];
  for (int spins = 0; spins < SPIN_CAP; ++spins) {
    unsigned top = 0;
    #pragma unroll
    for (int i = 0; i < 4; ++i) rx[i] = __builtin_amdgcn_raw_buffer_load_b128(rs, lane * 16 + i * 1024, WS_XC + (bx * 4 + wid) * 4096, 16);
    #pragma unroll
    for (int i = 0; i < 4; ++i) top = max(max(max(rx[i].x, rx[i].y), max(rx[i].z, rx[i].w)), top);
    if (__all(top != EMPTY)) break;
    asm volatile("s_sleep 1" ::: "memory");
  }
  STAMP(2, 3, hjob);
  const rsrc_t ro = __builtin_amdgcn_make_buffer_rsrc(out, 0, 4u << 20, 0x00020000);
  #pragma unroll
  for (int i = 0; i < 4; ++i) {
    u32x4 v;
    #pragma unroll
    for (int e = 0; e < 4; ++e) { const f32x2 t = own[i][e] + (f32x2){bflo(rx[i][e]), bfhi(rx[i][e])}; v[e] = cvtpk(t.x, t.y); }
    ldsw(lb + L_AUX + (wc * 32 + (i >> 1) * 16 + li) * 256 + ((wr * 8 + (i & 1) * 4 + g) ^ li) * 16, v);
  }
  #pragma unroll
  for (int i = 0; i < 4; ++i) {
    const int row = 8 * i + (lane >> 3);
    __builtin_amdgcn_raw_buffer_store_b128(ldsr(lb + L_AUX + (wc * 32 + row) * 256 + ((wr * 8 + (lane & 7)) ^ (row & 15)) * 16), ro, (srow + row) * 2048 + dt2 * 256 + wr * 128 + (lane & 7) * 16, 0, OUTPOL);
  }
  STAMP(2, 4, hjob);
  }
}

extern "C" void kernel_launch(void* const* d_in, const int* in_sizes, int n_in, void* d_out, int out_size, void* d_ws, size_t ws_size, hipStream_t stream) {
  (void)hipFuncSetAttribute((const void*)k_fused, hipFuncAttributeMaxDynamicSharedMemorySize, L_SIZE);
  k_fused<<<256, 512, L_SIZE, stream>>>((const char*)d_in[0], (const unsigned short*)d_in[1], (const unsigned short*)d_in[2], (const char*)d_in[4], (const unsigned short*)d_in[5],
                                        (const char*)d_in[6], (const unsigned short*)d_in[7], (char*)d_ws + ((ws_size - WS_END) & ~(size_t)0x1fffff), (char*)d_out);
}
```
